```python
import torch

def _softmax(x, axis=-1):
    return torch.softmax(x, dim=axis)
def _gelu(x):
    return torch.nn.functional.gelu(x, approximate="tanh")
def _silu(x):
    return torch.nn.functional.silu(x)
def _sigmoid(x):
    return torch.sigmoid(x)
def _layernorm(x, axis=-1, eps=1e-5):
    m = x.mean(dim=axis, keepdim=True)
    v = x.var(dim=axis, keepdim=True, unbiased=False)
    return (x - m) / torch.sqrt(v + eps)
def _scatter_add(tgt, idx, src, axis=0):
    return tgt.clone().index_add_(axis, idx, src)
def _segment_sum(src, seg, n):
    out = torch.zeros((n,) + src.shape[1:], dtype=src.dtype)
    return out.index_add_(0, seg, src)
def _pad(x, pw):
    flat = tuple(v for pair in reversed(pw) for v in pair)
    return torch.nn.functional.pad(x, flat)

kernel_name = 'standalone__fused_io_aware_attn__prompt'

def setup_inputs():

    _w = (lambda a: (lambda t: t.to(torch.bfloat16) if t.is_floating_point() else t)(torch.as_tensor(a)))
    B, H, S = 1, 32, 2048
    Dh = max(1, 512 // 32)
    return {
        "q": _w((lambda *s: torch.randn(*s, dtype=torch.bfloat16) * 0.8)(B, H, S, Dh)),
        "k": _w((lambda *s: torch.randn(*s, dtype=torch.bfloat16) * 0.8)(B, H, S, Dh)),
        "v": _w((lambda *s: torch.randn(*s, dtype=torch.bfloat16) * 0.05)(B, H, S, Dh)),
    }

def reference(q, k, v):
    scale = q.shape[-1] ** -0.5
    s = torch.einsum('bhqd,bhkd->bhqk', q, k) * scale
    p = _softmax(s, axis=-1)
    return torch.einsum('bhqk,bhkd->bhqd', p, v)
```

```cpp
#include <hip/hip_runtime.h>
#include <hip/hip_bf16.h>

typedef short  bf16x8 __attribute__((ext_vector_type(8)));
typedef short  s16x4  __attribute__((ext_vector_type(4)));
typedef float  f32x16 __attribute__((ext_vector_type(16)));
typedef float  f32x2  __attribute__((ext_vector_type(2)));
typedef __bf16 bf16x2 __attribute__((ext_vector_type(2)));
typedef unsigned u32x4 __attribute__((ext_vector_type(4)));
typedef __attribute__((address_space(3))) const char* lds_cptr;
#define DI __device__ __forceinline__
#define MFMA32(a, b, c) __builtin_amdgcn_mfma_f32_32x32x16_bf16((a), (b), (c), 0, 0, 0)

constexpr int NH = 32, SEQ = 2048, HD = 16;
constexpr int LDS_K = 0, LDS_V = 65536, LDS_ONES = 131072, LDS_BYTES = 131072 + 1024;
constexpr float CSCALE = 0.25f * 1.4426950408889634f;

DI unsigned cvtpk(float lo, float hi) { f32x2 v = {lo, hi}; bf16x2 b = __builtin_convertvector(v, bf16x2); return __builtin_bit_cast(unsigned, b); }
DI s16x4 vtr(lds_cptr p) { return __builtin_amdgcn_ds_read_tr16_b64_v4i16((__attribute__((address_space(3))) s16x4*)p); }
DI float halfmax(float m) { auto r = __builtin_amdgcn_permlane32_swap(__float_as_uint(m), __float_as_uint(m), false, false); return fmaxf(__uint_as_float(r[0]), __uint_as_float(r[1])); }

__global__ __launch_bounds__(512) void attn_kernel(const __hip_bfloat16* __restrict__ q, const __hip_bfloat16* __restrict__ k,
                                                   const __hip_bfloat16* __restrict__ v, __hip_bfloat16* __restrict__ out) {
  __shared__ __attribute__((aligned(1024))) char lds[LDS_BYTES];
  const int tid = threadIdx.x, lane = tid & 63, wid = tid >> 6;
  const int b = blockIdx.x, head = (b & 7) * 4 + (b >> 6), qblk = (b >> 3) & 7;
  const char* kg = (const char*)k + (size_t)head * 65536;
  const char* vg = (const char*)v + (size_t)head * 65536;
  for (int t = wid; t < 64; t += 8) {
    *(u32x4*)(lds + LDS_K + t * 1024 + lane * 16) = *(const u32x4*)(kg + t * 1024 + (lane & 31) * 32 + (lane >> 5) * 16);
    *(u32x4*)(lds + LDS_V + t * 1024 + lane * 16) = *(const u32x4*)(vg + t * 1024 + lane * 16);
  }
  if (tid < 256) *(unsigned*)(lds + LDS_ONES + tid * 4) = 0x3F803F80u;
  const size_t qrow = (size_t)head * SEQ + qblk * 256 + wid * 32 + (lane & 31);
  u32x4 qr = *(const u32x4*)((const char*)q + qrow * 32 + (lane >> 5) * 16);
  u32x4 qs;
#pragma unroll
  for (int i = 0; i < 4; ++i) qs[i] = cvtpk(__uint_as_float(qr[i] << 16) * CSCALE, __uint_as_float(qr[i] & 0xffff0000u) * CSCALE);
  const bf16x8 qf = __builtin_bit_cast(bf16x8, qs);
  __syncthreads();
  lds_cptr kp = (lds_cptr)lds + LDS_K + lane * 16;
  const int h = lane >> 5;
  const bool ones = (lane & 16) != 0;
  lds_cptr vp = (lds_cptr)lds + (ones ? LDS_ONES + (1 - h) * 128 : LDS_V + h * 128) + (lane & 15) * 8;
  const int vstride = ones ? 0 : 1024;
  f32x16 zero = {};
  float m = -INFINITY;
  for (int t = 0; t < 64; ++t) {
    bf16x8 kf = *(const __attribute__((address_space(3))) bf16x8*)(kp + t * 1024);
    f32x16 s = MFMA32(kf, qf, zero);
#pragma unroll
    for (int i = 0; i < 16; ++i) m = fmaxf(m, s[i]);
  }
  m = halfmax(m);
  f32x16 c;
#pragma unroll
  for (int i = 0; i < 16; ++i) c[i] = -m;
  f32x16 o = zero;
  for (int t = 0; t < 64; ++t) {
    bf16x8 kf = *(const __attribute__((address_space(3))) bf16x8*)(kp + t * 1024);
    f32x16 s = MFMA32(kf, qf, c);
    u32x4 p0, p1;
#pragma unroll
    for (int i = 0; i < 4; ++i) {
      p0[i] = cvtpk(__builtin_amdgcn_exp2f(s[2 * i]), __builtin_amdgcn_exp2f(s[2 * i + 1]));
      p1[i] = cvtpk(__builtin_amdgcn_exp2f(s[8 + 2 * i]), __builtin_amdgcn_exp2f(s[8 + 2 * i + 1]));
    }
    s16x4 a0 = vtr(vp), a1 = vtr(vp + 256), a2 = vtr(vp + 512), a3 = vtr(vp + 768);
    vp += vstride;
    o = MFMA32(__builtin_shufflevector(a0, a1, 0, 1, 2, 3, 4, 5, 6, 7), __builtin_bit_cast(bf16x8, p0), o);
    o = MFMA32(__builtin_shufflevector(a2, a3, 0, 1, 2, 3, 4, 5, 6, 7), __builtin_bit_cast(bf16x8, p1), o);
  }
  const float rl = 1.0f / o[8];
  unsigned a0 = cvtpk(o[0] * rl, o[1] * rl), a1 = cvtpk(o[2] * rl, o[3] * rl), b0 = cvtpk(o[4] * rl, o[5] * rl), b1 = cvtpk(o[6] * rl, o[7] * rl);
  auto r0 = __builtin_amdgcn_permlane32_swap(a0, b0, false, false);
  auto r1 = __builtin_amdgcn_permlane32_swap(a1, b1, false, false);
  u32x4 w = {r0[0], r1[0], r0[1], r1[1]};
  *(u32x4*)((char*)out + qrow * 32 + h * 16) = w;
}

extern "C" void kernel_launch(void* const* d_in, const int* in_sizes, int n_in, void* d_out, int out_size, void* d_ws, size_t ws_size, hipStream_t stream) {
  attn_kernel<<<256, 512, 0, stream>>>((const __hip_bfloat16*)d_in[0], (const __hip_bfloat16*)d_in[1], (const __hip_bfloat16*)d_in[2], (__hip_bfloat16*)d_out);
}
```

```cpp
#include <hip/hip_runtime.h>
#include <hip/hip_bf16.h>

typedef short  bf16x8 __attribute__((ext_vector_type(8)));
typedef short  s16x4  __attribute__((ext_vector_type(4)));
typedef float  f32x16 __attribute__((ext_vector_type(16)));
typedef float  f32x2  __attribute__((ext_vector_type(2)));
typedef __bf16 bf16x2 __attribute__((ext_vector_type(2)));
typedef unsigned u32x4 __attribute__((ext_vector_type(4)));
typedef __attribute__((address_space(3))) const char* lds_cptr;
#define DI __device__ __forceinline__
#define MFMA32(a, b, c) __builtin_amdgcn_mfma_f32_32x32x16_bf16((a), (b), (c), 0, 0, 0)

constexpr int LDS_K = 0, LDS_V = 65536, LDS_ONES = 131072, LDS_BYTES = 157952;
constexpr float CSCALE = 0.25f * 1.4426950408889634f;

DI unsigned cvtpk(float lo, float hi) { f32x2 v = {lo, hi}; bf16x2 b = __builtin_convertvector(v, bf16x2); return __builtin_bit_cast(unsigned, b); }
DI s16x4 vtr(lds_cptr p) { return __builtin_amdgcn_ds_read_tr16_b64_v4i16((__attribute__((address_space(3))) s16x4*)p); }
DI float halfmax(float m) { auto r = __builtin_amdgcn_permlane32_swap(__float_as_uint(m), __float_as_uint(m), false, false); return fmaxf(__uint_as_float(r[0]), __uint_as_float(r[1])); }

DI f32x16 exact_tile(const char* qrow, lds_cptr lds, int lane) {
  const bf16x8 qf = *(const bf16x8*)qrow;
  lds_cptr kp = lds + LDS_K + lane * 16;
  const int h = lane >> 5;
  const bool ones = (lane & 16) != 0;
  lds_cptr vp = lds + (ones ? LDS_ONES + (1 - h) * 128 + (lane & 3) * 8 : LDS_V + h * 128 + (lane & 15) * 8);
  const int vstride = ones ? 0 : 1024;
  const f32x16 zero = {};
  float m = -INFINITY;
#pragma unroll 1
  for (int t = 0; t < 64; ++t) {
    const bf16x8 kf = *(const __attribute__((address_space(3))) bf16x8*)(kp + t * 1024);
    const f32x16 s = MFMA32(kf, qf, zero);
#pragma unroll
    for (int i = 0; i < 16; ++i) m = fmaxf(m, s[i]);
  }
  m = halfmax(m);
  f32x16 c;
#pragma unroll
  for (int i = 0; i < 16; ++i) c[i] = -m;
  f32x16 o = zero;
#pragma unroll 1
  for (int t = 0; t < 64; ++t) {
    const bf16x8 kf = *(const __attribute__((address_space(3))) bf16x8*)(kp + t * 1024);
    const f32x16 s = MFMA32(kf, qf, c);
    u32x4 p0, p1;
#pragma unroll
    for (int i = 0; i < 4; ++i) {
      p0[i] = cvtpk(__builtin_amdgcn_exp2f(s[2 * i] * CSCALE), __builtin_amdgcn_exp2f(s[2 * i + 1] * CSCALE));
      p1[i] = cvtpk(__builtin_amdgcn_exp2f(s[8 + 2 * i] * CSCALE), __builtin_amdgcn_exp2f(s[8 + 2 * i + 1] * CSCALE));
    }
    const s16x4 a0 = vtr(vp), a1 = vtr(vp + 256), a2 = vtr(vp + 512), a3 = vtr(vp + 768);
    vp += vstride;
    o = MFMA32(__builtin_shufflevector(a0, a1, 0, 1, 2, 3, 4, 5, 6, 7), __builtin_bit_cast(bf16x8, p0), o);
    o = MFMA32(__builtin_shufflevector(a2, a3, 0, 1, 2, 3, 4, 5, 6, 7), __builtin_bit_cast(bf16x8, p1), o);
  }
  return o;
}

DI void exact_store(const char* qrow, char* orow, lds_cptr lds, int lane) {
  const f32x16 o = exact_tile(qrow, lds, lane);
  const float rl = __builtin_amdgcn_rcpf(o[8]);
  const unsigned a0 = cvtpk(o[0] * rl, o[1] * rl), a1 = cvtpk(o[2] * rl, o[3] * rl), b0 = cvtpk(o[4] * rl, o[5] * rl), b1 = cvtpk(o[6] * rl, o[7] * rl);
  const auto r0 = __builtin_amdgcn_permlane32_swap(a0, b0, false, false);
  const auto r1 = __builtin_amdgcn_permlane32_swap(a1, b1, false, false);
  *(u32x4*)orow = (u32x4){r0[0], r1[0], r0[1], r1[1]};
}

__global__ __launch_bounds__(768) void attn_kernel(const char* __restrict__ q, const char* __restrict__ k, const char* __restrict__ v, char* __restrict__ out) {
  __shared__ __attribute__((aligned(1024))) char lds[LDS_BYTES];
  const unsigned tid = threadIdx.x, bid = blockIdx.x, lds0 = (unsigned)(size_t)lds;
  const auto karg = __builtin_amdgcn_kernarg_segment_ptr();
  const char* qp; char* op; unsigned qoff, lane, wid, bad;
  asm volatile(
      "s_load_dwordx8 s[20:27], %[karg], 0x0\n"
      "v_readfirstlane_b32 s40, v0\n"
      "v_and_b32 v9, 63, v0\n"
      "s_and_b32 s41, %[bid], 31\n"
      "v_and_b32 v4, 31, v0\n"
      "s_lshr_b32 s40, s40, 6\n"
      "v_lshrrev_b32 v7, 5, v9\n"
      "s_lshl_b32 s41, s41, 16\n"
      "v_lshlrev_b32 v4, 5, v4\n"
      "s_lshr_b32 s42, %[bid], 5\n"
      "v_lshl_or_b32 v4, v7, 4, v4\n"
      "s_lshl_b32 s42, s42, 13\n"
      "s_add_u32 s42, s42, s41\n"
      "s_cmp_lt_u32 s40, 4\n"
      "s_cbranch_scc1 Lldr%=\n"
      "s_sub_u32 s40, s40, 4\n"
      "s_and_b32 s43, s40, 3\n"
      "s_lshr_b32 s44, s40, 2\n"
      "s_lshl_b32 s45, s43, 11\n"
      "s_add_u32 s42, s42, s45\n"
      "s_lshl_b32 s45, s44, 10\n"
      "s_add_u32 s42, s42, s45\n"
      "v_add_u32 v5, s42, v4\n"
      "s_lshl_b32 s45, s44, 15\n"
      "s_add_u32 s45, s45, s41\n"
      "v_add_u32 v4, s45, v4\n"
      "s_waitcnt lgkmcnt(0)\n"
      "global_load_dwordx4 v[10:13], v5, s[20:21] nt\n"
      "global_load_dwordx4 v[18:21], v4, s[22:23]\n"
      "s_branch Lcomp%=\n"
      "Lldr%=:\n"
      "s_lshr_b32 s44, s40, 1\n"
      "v_lshlrev_b32 v9, 4, v9\n"
      "s_getpc_b64 s[52:53]\n"
      "Lpc%=:\n"
      "s_lshr_b32 s54, %[bid], 3\n"
      "s_lshl_b32 s55, s40, 2\n"
      "s_add_u32 s54, s54, s55\n"
      "s_and_b32 s54, s54, 15\n"
      "s_lshl_b32 s54, s54, 10\n"
      "v_add_u32 v10, s54, v9\n"
      "s_mov_b32 s54, Lfin%=-Lpc%=\n"
      "v_min_u32 v10, s54, v10\n"
      "global_load_dword v11, v10, s[52:53]\n"
      "s_lshl_b32 s45, s44, 15\n"
      "s_add_u32 s43, s41, s45\n"
      "s_bitcmp1_b32 s40, 0\n"
      "s_cselect_b64 vcc, -1, 0\n"
      "s_cselect_b32 s47, 65536, 0\n"
      "v_cndmask_b32 v4, v4, v9, vcc\n"
      "v_add_u32 v4, s43, v4\n"
      "s_waitcnt lgkmcnt(0)\n"
      "s_cselect_b32 s48, s24, s22\n"
      "s_cselect_b32 s49, s25, s23\n"
      "global_load_dwordx4 v[16:19], v4, s[48:49] offset:0\n"
      "global_load_dwordx4 v[20:23], v4, s[48:49] offset:1024\n"
      "s_lshl_b32 s54, s44, 6\n"
      "s_add_u32 s55, %[lds0], 157696\n"
      "s_add_u32 s54, s54, s55\n"
      "s_and_b32 s46, s40, 1\n"
      "s_add_u32 s54, s54, s46\n"
      "v_mov_b32 v6, s54\n"
      "v_mov_b32 v13, 0\n"
      "v_mov_b32 v15, 0\n"
      "v_mov_b32 v12, 0x3f80\n"
      "v_lshrrev_b32 v14, 2, v9\n"
      "v_add_u32 v14, s55, v14\n"
      "ds_write_b32 v14, v13\n"
      "v_and_b32 v14, 1, v0\n"
      "v_cmp_eq_u32 vcc, 0, v14\n"
      "s_add_u32 s55, %[lds0], 131072\n"
      "v_lshlrev_b32 v3, 4, v0\n"
      "v_add_u32 v3, s55, v3\n"
      "v_cndmask_b32 v12, 0, v12, vcc\n"
      "s_add_u32 s47, s47, s45\n"
      "v_mov_b32 v14, v12\n"
      "s_add_u32 s47, s47, %[lds0]\n"
      "ds_write_b128 v3, v[12:15] offset:0\n"
      "ds_write_b128 v3, v[12:15] offset:4096\n"
      "v_add_u32 v1, s47, v9\n"
      "v_mov_b32 v8, 1\n"
      "v_mov_b32 v5, s42\n"
      "s_waitcnt lgkmcnt(0)\n"
      "s_barrier\n"
      "global_load_dwordx4 v[24:27], v4, s[48:49] offset:2048\n"
      "global_load_dwordx4 v[28:31], v4, s[48:49] offset:3072\n"
      "s_waitcnt vmcnt(3)\n"
      "ds_write_b128 v1, v[16:19] offset:0\n"
      "s_add_u32 s48, s48, 4096\n"
      "s_addc_u32 s49, s49, 0\n"
      "global_load_dwordx4 v[32:35], v4, s[48:49] offset:0\n"
      "s_waitcnt vmcnt(3)\n"
      "ds_write_b128 v1, v[20:23] offset:1024\n"
      "ds_write_b8 v6, v8 offset:0\n"
      "global_load_dwordx4 v[36:39], v4, s[48:49] offset:1024\n"
      "s_waitcnt vmcnt(3)\n"
      "ds_write_b128 v1, v[24:27] offset:2048\n"
      "global_load_dwordx4 v[40:43], v4, s[48:49] offset:2048\n"
      "s_waitcnt vmcnt(3)\n"
      "ds_write_b128 v1, v[28:31] offset:3072\n"
      "ds_write_b8 v6, v8 offset:4\n"
      "global_load_dwordx4 v[44:47], v4, s[48:49] offset:3072\n"
      "s_sleep 3\n"
      "s_branch Lldr2%=\n"
      "Lcomp%=:\n"
      "s_lshl_b32 s45, s44, 15\n"
      "s_add_u32 s45, s45, %[lds0]\n"
      "v_lshl_add_u32 v1, v9, 4, s45\n"
      "s_add_u32 s42, s45, 65536\n"
      "v_and_b32 v2, 15, v9\n"
      "v_lshlrev_b32 v2, 3, v2\n"
      "v_lshl_add_u32 v2, v7, 8, v2\n"
      "v_add_u32 v2, s42, v2\n"
      "s_add_u32 s42, %[lds0], 131200\n"
      "v_and_b32 v8, 3, v9\n"
      "v_lshlrev_b32 v8, 3, v8\n"
      "v_add_u32 v8, s42, v8\n"
      "v_and_b32 v3, 16, v9\n"
      "v_cmp_eq_u32 vcc, 0, v3\n"
      "v_mov_b32 v3, 8192\n"
      "s_lshl_b32 s42, s44, 6\n"
      "v_cndmask_b32 v2, v8, v2, vcc\n"
      "v_cndmask_b32 v3, 0, v3, vcc\n"
      "s_add_u32 s46, %[lds0], 157696\n"
      "s_add_u32 s42, s42, s46\n"
      "v_mov_b32 v6, s42\n"
      "s_lshl_b32 s42, s40, 2\n"
      "s_add_u32 s42, s42, s46\n"
      "v_mov_b32 v149, s42\n"
      "s_xor_b32 s42, s42, 16\n"
      "v_mov_b32 v148, s42\n"
      "v_cvt_f32_u32 v7, v7\n"
      "v_add_f32 v7, -1.0, v7\n"
      "v_mov_b32 v42, 0x3eb8aa3b\n"
      "v_mov_b32 v43, 0x3eb8aa3b\n"
      "v_mov_b32 v44, 0xdddddddd\n"
      "v_mov_b32 v78, 0\n"
      "v_mov_b32 v79, 0\n"
      "v_mov_b32 v80, 0\n"
      "v_mov_b32 v81, 0\n"
      "v_mov_b32 v82, 0\n"
      "v_mov_b32 v83, 0\n"
      "v_mov_b32 v84, 0\n"
      "v_mov_b32 v85, 0\n"
      "v_mov_b32 v86, 0\n"
      "v_mov_b32 v87, 0\n"
      "v_mov_b32 v88, 0\n"
      "v_mov_b32 v89, 0\n"
      "v_mov_b32 v90, 0\n"
      "v_mov_b32 v91, 0\n"
      "v_mov_b32 v92, 0\n"
      "v_mov_b32 v93, 0\n"
      "v_mov_b32 v94, 0\n"
      "v_mov_b32 v95, 0\n"
      "v_mov_b32 v96, 0\n"
      "v_mov_b32 v97, 0\n"
      "v_mov_b32 v98, 0\n"
      "v_mov_b32 v99, 0\n"
      "v_mov_b32 v100, 0\n"
      "v_mov_b32 v101, 0\n"
      "v_mov_b32 v102, 0\n"
      "v_mov_b32 v103, 0\n"
      "v_mov_b32 v104, 0\n"
      "v_mov_b32 v105, 0\n"
      "v_mov_b32 v106, 0\n"
      "v_mov_b32 v107, 0\n"
      "v_mov_b32 v108, 0\n"
      "v_mov_b32 v109, 0\n"
      "s_add_u32 s46, %[lds0], 139264\n"
      "s_mul_i32 s42, s40, 2304\n"
      "s_xor_b32 s43, s40, 4\n"
      "s_mul_i32 s43, s43, 2304\n"
      "s_add_u32 s42, s42, s46\n"
      "s_add_u32 s43, s43, s46\n"
      "v_and_b32 v147, 31, v9\n"
      "v_lshl_add_u32 v142, v9, 4, s42\n"
      "v_lshl_add_u32 v143, v9, 2, s42\n"
      "v_lshl_add_u32 v144, v9, 4, s43\n"
      "v_lshl_add_u32 v145, v9, 2, s43\n"
      "v_lshl_add_u32 v146, v147, 4, s43\n"
      "v_lshl_add_u32 v147, v147, 4, s42\n"
      "v_xor_b32 v5, 1024, v5\n"
      "s_barrier\n"
      "s_setprio 3\n"
      "s_waitcnt vmcnt(1)\n"
      "v_lshlrev_b32 v46, 16, v10\n"
      "v_and_b32 v47, 0xffff0000, v10\n"
      "v_lshlrev_b32 v48, 16, v11\n"
      "v_and_b32 v49, 0xffff0000, v11\n"
      "v_lshlrev_b32 v50, 16, v12\n"
      "v_and_b32 v51, 0xffff0000, v12\n"
      "v_lshlrev_b32 v52, 16, v13\n"
      "v_and_b32 v53, 0xffff0000, v13\n"
      "v_pk_mul_f32 v[46:47], v[46:47], v[42:43]\n"
      "v_pk_mul_f32 v[48:49], v[48:49], v[42:43]\n"
      "v_pk_mul_f32 v[50:51], v[50:51], v[42:43]\n"
      "v_pk_mul_f32 v[52:53], v[52:53], v[42:43]\n"
      "v_cvt_pk_bf16_f32 v10, v46, v47\n"
      "v_cvt_pk_bf16_f32 v11, v48, v49\n"
      "v_cvt_pk_bf16_f32 v12, v50, v51\n"
      "v_cvt_pk_bf16_f32 v13, v52, v53\n"
      "s_waitcnt vmcnt(0)\n"
      "ds_write_b128 v144, v[10:13]\n"
      "v_mfma_f32_32x32x16_bf16 v[110:125], v[18:21], v[10:13], 0\n"
      "s_nop 11\n"
      "v_max3_f32 v110, v110, v111, v112\n"
      "v_max3_f32 v113, v113, v114, v115\n"
      "v_max3_f32 v116, v116, v117, v118\n"
      "v_max3_f32 v119, v119, v120, v121\n"
      "v_max3_f32 v122, v122, v123, v124\n"
      "v_max3_f32 v110, v110, v113, v125\n"
      "v_max3_f32 v116, v116, v119, v122\n"
      "v_max_f32 v110, v110, v116\n"
      "s_nop 1\n"
      "v_mfma_f32_32x32x2_f32 v[46:61], v7, v110, 0\n"
      "v_mfma_f32_32x32x16_bf16 v[110:125], v[18:21], v[10:13], v[46:61]\n"
      "v_mov_b32 v4, 1\n"
      "s_nop 0\n"
      "ds_read_b32 v8, v6 offset:0\n"
      "ds_read_b128 v[22:25], v1 offset:1024\n"
      "ds_read_b64_tr_b16 v[26:27], v2 offset:0\n"
      "ds_read_b64_tr_b16 v[28:29], v2 offset:128\n"
      "ds_read_b64_tr_b16 v[30:31], v2 offset:512\n"
      "ds_read_b64_tr_b16 v[32:33], v2 offset:640\n"
      "ds_read_b64_tr_b16 v[34:35], v2 offset:1024\n"
      "ds_read_b64_tr_b16 v[36:37], v2 offset:1152\n"
      "ds_read_b64_tr_b16 v[38:39], v2 offset:1536\n"
      "ds_read_b64_tr_b16 v[40:41], v2 offset:1664\n"
      "v_exp_f32 v110, v110\n"
      "v_exp_f32 v111, v111\n"
      "v_exp_f32 v112, v112\n"
      "v_exp_f32 v113, v113\n"
      "v_exp_f32 v114, v114\n"
      "v_exp_f32 v115, v115\n"
      "v_exp_f32 v116, v116\n"
      "ds_write_b128 v146, v[46:49] offset:1024\n"
      "ds_write_b32 v148, v4 offset:128\n"
      "s_setprio 2\n"
      "v_exp_f32 v117, v117\n"
      "v_exp_f32 v118, v118\n"
      "v_exp_f32 v119, v119\n"
      "s_waitcnt lgkmcnt(0)\n"
      "v_readfirstlane_b32 s46, v8\n"
      "s_cmp_eq_u32 s46, 257\n"
      "s_cbranch_scc0 Lsa_%=\n"
      "Lba_%=:\n"
      "v_mfma_f32_32x32x16_bf16 v[126:141], v[22:25], v[10:13], v[46:61]\n"
      "v_exp_f32 v120, v120\n"
      "v_exp_f32 v121, v121\n"
      "v_exp_f32 v122, v122\n"
      "v_smfmac_f32_32x32x32_bf16 v[78:93], v[26:29], v[110:117], v44\n"
      "v_exp_f32 v123, v123\n"
      "v_exp_f32 v124, v124\n"
      "v_exp_f32 v125, v125\n"
      "s_nop 4\n"
      "v_exp_f32 v126, v126\n"
      "v_exp_f32 v127, v127\n"
      "v_smfmac_f32_32x32x32_bf16 v[78:93], v[30:33], v[118:125], v44\n"
      "ds_read_b32 v8, v149 offset:128\n"
      "ds_read_b128 v[14:17], v142\n"
      "ds_read_b128 v[62:65], v147 offset:1024\n"
      "ds_read_b128 v[66:69], v147 offset:1024\n"
      "ds_read_b128 v[70:73], v147 offset:1024\n"
      "ds_read_b128 v[74:77], v147 offset:1024\n"
      "v_exp_f32 v128, v128\n"
      "v_exp_f32 v129, v129\n"
      "v_exp_f32 v130, v130\n"
      "v_exp_f32 v131, v131\n"
      "v_exp_f32 v132, v132\n"
      "v_exp_f32 v133, v133\n"
      "v_exp_f32 v134, v134\n"
      "v_exp_f32 v135, v135\n"
      "v_smfmac_f32_32x32x32_bf16 v[78:93], v[34:37], v[126:133], v44\n"
      "v_exp_f32 v136, v136\n"
      "v_exp_f32 v137, v137\n"
      "v_exp_f32 v138, v138\n"
      "v_exp_f32 v139, v139\n"
      "s_waitcnt lgkmcnt(0)\n"
      "v_readfirstlane_b32 s46, v8\n"
      "s_cmp_ge_u32 s46, 1\n"
      "s_cbranch_scc0 Lsb_%=\n"
      "Lbb_%=:\n"
      "v_mfma_f32_32x32x16_bf16 v[110:125], v[18:21], v[14:17], v[62:77]\n"
      "v_exp_f32 v140, v140\n"
      "v_exp_f32 v141, v141\n"
      "s_nop 9\n"
      "s_setprio 3\n"
      "v_exp_f32 v110, v110\n"
      "v_exp_f32 v111, v111\n"
      "v_smfmac_f32_32x32x32_bf16 v[78:93], v[38:41], v[134:141], v44\n"
      "v_exp_f32 v112, v112\n"
      "v_exp_f32 v113, v113\n"
      "v_exp_f32 v114, v114\n"
      "v_mfma_f32_32x32x16_bf16 v[126:141], v[22:25], v[14:17], v[62:77]\n"
      "v_exp_f32 v115, v115\n"
      "v_exp_f32 v116, v116\n"
      "v_exp_f32 v117, v117\n"
      "v_exp_f32 v118, v118\n"
      "v_exp_f32 v119, v119\n"
      "ds_read_b32 v8, v6 offset:4\n"
      "ds_read_b128 v[18:21], v1 offset:2048\n"
      "v_smfmac_f32_32x32x32_bf16 v[94:109], v[26:29], v[110:117], v44\n"
      "v_exp_f32 v120, v120\n"
      "v_exp_f32 v121, v121\n"
      "v_exp_f32 v122, v122\n"
      "v_exp_f32 v123, v123\n"
      "v_exp_f32 v124, v124\n"
      "v_exp_f32 v125, v125\n"
      "s_waitcnt lgkmcnt(0)\n"
      "v_readfirstlane_b32 s46, v8\n"
      "s_cmp_eq_u32 s46, 257\n"
      "s_cbranch_scc0 Lsc_%=\n"
      "Lbc_%=:\n"
      "v_exp_f32 v126, v126\n"
      "v_exp_f32 v127, v127\n"
      "v_smfmac_f32_32x32x32_bf16 v[94:109], v[30:33], v[118:125], v44\n"
      "v_exp_f32 v128, v128\n"
      "v_exp_f32 v129, v129\n"
      "v_exp_f32 v130, v130\n"
      "v_mfma_f32_32x32x16_bf16 v[110:125], v[18:21], v[10:13], v[46:61]\n"
      "v_exp_f32 v131, v131\n"
      "v_exp_f32 v132, v132\n"
      "v_exp_f32 v133, v133\n"
      "v_exp_f32 v134, v134\n"
      "v_exp_f32 v135, v135\n"
      "v_smfmac_f32_32x32x32_bf16 v[94:109], v[34:37], v[126:133], v44\n"
      "v_exp_f32 v136, v136\n"
      "v_exp_f32 v137, v137\n"
      "v_exp_f32 v138, v138\n"
      "v_exp_f32 v139, v139\n"
      "v_exp_f32 v140, v140\n"
      "v_exp_f32 v141, v141\n"
      "ds_read_b64_tr_b16 v[26:27], v2 offset:2048\n"
      "ds_read_b64_tr_b16 v[28:29], v2 offset:2176\n"
      "ds_read_b64_tr_b16 v[30:31], v2 offset:2560\n"
      "ds_read_b64_tr_b16 v[32:33], v2 offset:2688\n"
      "ds_read_b128 v[22:25], v1 offset:3072\n"
      "ds_read_b32 v8, v6 offset:8\n"
      "v_exp_f32 v110, v110\n"
      "v_exp_f32 v111, v111\n"
      "v_smfmac_f32_32x32x32_bf16 v[94:109], v[38:41], v[134:141], v44\n"
      "v_exp_f32 v112, v112\n"
      "v_exp_f32 v113, v113\n"
      "v_exp_f32 v114, v114\n"
      "v_mfma_f32_32x32x16_bf16 v[126:141], v[18:21], v[14:17], v[62:77]\n"
      "v_exp_f32 v115, v115\n"
      "v_exp_f32 v116, v116\n"
      "v_exp_f32 v117, v117\n"
      "v_exp_f32 v118, v118\n"
      "v_exp_f32 v119, v119\n"
      "s_waitcnt lgkmcnt(0)\n"
      "v_smfmac_f32_32x32x32_bf16 v[78:93], v[26:29], v[110:117], v44\n"
      "v_exp_f32 v120, v120\n"
      "v_exp_f32 v121, v121\n"
      "v_exp_f32 v122, v122\n"
      "v_exp_f32 v123, v123\n"
      "v_exp_f32 v124, v124\n"
      "v_exp_f32 v125, v125\n"
      "v_readfirstlane_b32 s46, v8\n"
      "s_cmp_eq_u32 s46, 257\n"
      "s_cbranch_scc0 Lpm2_%=\n"
      "Lrm2_%=:\n"
      "ds_read_b64_tr_b16 v[34:35], v2 offset:3072\n"
      "ds_read_b64_tr_b16 v[36:37], v2 offset:3200\n"
      "ds_read_b64_tr_b16 v[38:39], v2 offset:3584\n"
      "ds_read_b64_tr_b16 v[40:41], v2 offset:3712\n"
      "ds_read_b128 v[18:21], v1 offset:4096\n"
      "v_exp_f32 v126, v126\n"
      "v_exp_f32 v127, v127\n"
      "v_smfmac_f32_32x32x32_bf16 v[78:93], v[30:33], v[118:125], v44\n"
      "v_exp_f32 v128, v128\n"
      "v_exp_f32 v129, v129\n"
      "v_exp_f32 v130, v130\n"
      "v_mfma_f32_32x32x16_bf16 v[110:125], v[22:25], v[10:13], v[46:61]\n"
      "v_exp_f32 v131, v131\n"
      "v_exp_f32 v132, v132\n"
      "v_exp_f32 v133, v133\n"
      "v_exp_f32 v134, v134\n"
      "v_exp_f32 v135, v135\n"
      "v_smfmac_f32_32x32x32_bf16 v[94:109], v[26:29], v[126:133], v44\n"
      "v_exp_f32 v136, v136\n"
      "v_exp_f32 v137, v137\n"
      "v_exp_f32 v138, v138\n"
      "v_exp_f32 v139, v139\n"
      "v_exp_f32 v140, v140\n"
      "v_exp_f32 v141, v141\n"
      "s_waitcnt lgkmcnt(0)\n"
      "v_exp_f32 v110, v110\n"
      "v_exp_f32 v111, v111\n"
      "v_smfmac_f32_32x32x32_bf16 v[94:109], v[30:33], v[134:141], v44\n"
      "v_exp_f32 v112, v112\n"
      "v_exp_f32 v113, v113\n"
      "v_exp_f32 v114, v114\n"
      "v_mfma_f32_32x32x16_bf16 v[126:141], v[22:25], v[14:17], v[62:77]\n"
      "v_exp_f32 v115, v115\n"
      "v_exp_f32 v116, v116\n"
      "v_exp_f32 v117, v117\n"
      "v_exp_f32 v118, v118\n"
      "v_exp_f32 v119, v119\n"
      "v_smfmac_f32_32x32x32_bf16 v[78:93], v[34:37], v[110:117], v44\n"
      "v_exp_f32 v120, v120\n"
      "v_exp_f32 v121, v121\n"
      "v_exp_f32 v122, v122\n"
      "v_exp_f32 v123, v123\n"
      "v_exp_f32 v124, v124\n"
      "v_exp_f32 v125, v125\n"
      "ds_read_b64_tr_b16 v[26:27], v2 offset:4096\n"
      "ds_read_b64_tr_b16 v[28:29], v2 offset:4224\n"
      "ds_read_b64_tr_b16 v[30:31], v2 offset:4608\n"
      "ds_read_b64_tr_b16 v[32:33], v2 offset:4736\n"
      "ds_read_b128 v[22:25], v1 offset:5120\n"
      "ds_read_b32 v8, v6 offset:12\n"
      "v_exp_f32 v126, v126\n"
      "v_exp_f32 v127, v127\n"
      "v_smfmac_f32_32x32x32_bf16 v[78:93], v[38:41], v[118:125], v44\n"
      "v_exp_f32 v128, v128\n"
      "v_exp_f32 v129, v129\n"
      "v_exp_f32 v130, v130\n"
      "v_mfma_f32_32x32x16_bf16 v[110:125], v[18:21], v[10:13], v[46:61]\n"
      "v_exp_f32 v131, v131\n"
      "v_exp_f32 v132, v132\n"
      "v_exp_f32 v133, v133\n"
      "v_exp_f32 v134, v134\n"
      "v_exp_f32 v135, v135\n"
      "v_smfmac_f32_32x32x32_bf16 v[94:109], v[34:37], v[126:133], v44\n"
      "v_exp_f32 v136, v136\n"
      "v_exp_f32 v137, v137\n"
      "v_exp_f32 v138, v138\n"
      "v_exp_f32 v139, v139\n"
      "v_exp_f32 v140, v140\n"
      "v_exp_f32 v141, v141\n"
      "s_waitcnt lgkmcnt(0)\n"
      "v_exp_f32 v110, v110\n"
      "v_exp_f32 v111, v111\n"
      "v_smfmac_f32_32x32x32_bf16 v[94:109], v[38:41], v[134:141], v44\n"
      "v_exp_f32 v112, v112\n"
      "v_exp_f32 v113, v113\n"
      "v_exp_f32 v114, v114\n"
      "v_mfma_f32_32x32x16_bf16 v[126:141], v[18:21], v[14:17], v[62:77]\n"
      "v_exp_f32 v115, v115\n"
      "v_exp_f32 v116, v116\n"
      "v_exp_f32 v117, v117\n"
      "v_exp_f32 v118, v118\n"
      "v_exp_f32 v119, v119\n"
      "v_smfmac_f32_32x32x32_bf16 v[78:93], v[26:29], v[110:117], v44\n"
      "v_exp_f32 v120, v120\n"
      "v_exp_f32 v121, v121\n"
      "v_exp_f32 v122, v122\n"
      "v_exp_f32 v123, v123\n"
      "v_exp_f32 v124, v124\n"
      "v_exp_f32 v125, v125\n"
      "v_readfirstlane_b32 s46, v8\n"
      "s_cmp_eq_u32 s46, 257\n"
      "s_cbranch_scc0 Lpm3_%=\n"
      "Lrm3_%=:\n"
      "ds_read_b64_tr_b16 v[34:35], v2 offset:5120\n"
      "ds_read_b64_tr_b16 v[36:37], v2 offset:5248\n"
      "ds_read_b64_tr_b16 v[38:39], v2 offset:5632\n"
      "ds_read_b64_tr_b16 v[40:41], v2 offset:5760\n"
      "ds_read_b128 v[18:21], v1 offset:6144\n"
      "v_exp_f32 v126, v126\n"
      "v_exp_f32 v127, v127\n"
      "v_smfmac_f32_32x32x32_bf16 v[78:93], v[30:33], v[118:125], v44\n"
      "v_exp_f32 v128, v128\n"
      "v_exp_f32 v129, v129\n"
      "v_exp_f32 v130, v130\n"
      "v_mfma_f32_32x32x16_bf16 v[110:125], v[22:25], v[10:13], v[46:61]\n"
      "v_exp_f32 v131, v131\n"
      "v_exp_f32 v132, v132\n"
      "v_exp_f32 v133, v133\n"
      "v_exp_f32 v134, v134\n"
      "v_exp_f32 v135, v135\n"
      "v_smfmac_f32_32x32x32_bf16 v[94:109], v[26:29], v[126:133], v44\n"
      "v_exp_f32 v136, v136\n"
      "v_exp_f32 v137, v137\n"
      "v_exp_f32 v138, v138\n"
      "v_exp_f32 v139, v139\n"
      "v_exp_f32 v140, v140\n"
      "v_exp_f32 v141, v141\n"
      "s_waitcnt lgkmcnt(0)\n"
      "v_exp_f32 v110, v110\n"
      "v_exp_f32 v111, v111\n"
      "v_smfmac_f32_32x32x32_bf16 v[94:109], v[30:33], v[134:141], v44\n"
      "v_exp_f32 v112, v112\n"
      "v_exp_f32 v113, v113\n"
      "v_exp_f32 v114, v114\n"
      "v_mfma_f32_32x32x16_bf16 v[126:141], v[22:25], v[14:17], v[62:77]\n"
      "v_exp_f32 v115, v115\n"
      "v_exp_f32 v116, v116\n"
      "v_exp_f32 v117, v117\n"
      "v_exp_f32 v118, v118\n"
      "v_exp_f32 v119, v119\n"
      "v_smfmac_f32_32x32x32_bf16 v[78:93], v[34:37], v[110:117], v44\n"
      "v_exp_f32 v120, v120\n"
      "v_exp_f32 v121, v121\n"
      "v_exp_f32 v122, v122\n"
      "v_exp_f32 v123, v123\n"
      "v_exp_f32 v124, v124\n"
      "v_exp_f32 v125, v125\n"
      "ds_read_b64_tr_b16 v[26:27], v2 offset:6144\n"
      "ds_read_b64_tr_b16 v[28:29], v2 offset:6272\n"
      "ds_read_b64_tr_b16 v[30:31], v2 offset:6656\n"
      "ds_read_b64_tr_b16 v[32:33], v2 offset:6784\n"
      "ds_read_b128 v[22:25], v1 offset:7168\n"
      "v_exp_f32 v126, v126\n"
      "v_exp_f32 v127, v127\n"
      "v_smfmac_f32_32x32x32_bf16 v[78:93], v[38:41], v[118:125], v44\n"
      "v_exp_f32 v128, v128\n"
      "v_exp_f32 v129, v129\n"
      "v_exp_f32 v130, v130\n"
      "v_mfma_f32_32x32x16_bf16 v[110:125], v[18:21], v[10:13], v[46:61]\n"
      "v_exp_f32 v131, v131\n"
      "v_exp_f32 v132, v132\n"
      "v_exp_f32 v133, v133\n"
      "v_exp_f32 v134, v134\n"
      "v_exp_f32 v135, v135\n"
      "v_smfmac_f32_32x32x32_bf16 v[94:109], v[34:37], v[126:133], v44\n"
      "v_exp_f32 v136, v136\n"
      "v_exp_f32 v137, v137\n"
      "v_exp_f32 v138, v138\n"
      "v_exp_f32 v139, v139\n"
      "v_exp_f32 v140, v140\n"
      "v_exp_f32 v141, v141\n"
      "s_waitcnt lgkmcnt(0)\n"
      "v_exp_f32 v110, v110\n"
      "v_exp_f32 v111, v111\n"
      "v_smfmac_f32_32x32x32_bf16 v[94:109], v[38:41], v[134:141], v44\n"
      "v_exp_f32 v112, v112\n"
      "v_exp_f32 v113, v113\n"
      "v_exp_f32 v114, v114\n"
      "v_mfma_f32_32x32x16_bf16 v[126:141], v[18:21], v[14:17], v[62:77]\n"
      "v_exp_f32 v115, v115\n"
      "v_exp_f32 v116, v116\n"
      "v_exp_f32 v117, v117\n"
      "v_exp_f32 v118, v118\n"
      "v_exp_f32 v119, v119\n"
      "v_smfmac_f32_32x32x32_bf16 v[78:93], v[26:29], v[110:117], v44\n"
      "v_exp_f32 v120, v120\n"
      "v_exp_f32 v121, v121\n"
      "v_exp_f32 v122, v122\n"
      "v_exp_f32 v123, v123\n"
      "v_exp_f32 v124, v124\n"
      "v_exp_f32 v125, v125\n"
      "ds_read_b64_tr_b16 v[34:35], v2 offset:7168\n"
      "ds_read_b64_tr_b16 v[36:37], v2 offset:7296\n"
      "ds_read_b64_tr_b16 v[38:39], v2 offset:7680\n"
      "ds_read_b64_tr_b16 v[40:41], v2 offset:7808\n"
      "ds_read_b128 v[18:21], v1 offset:8192\n"
      "ds_read_b32 v8, v6 offset:16\n"
      "v_exp_f32 v126, v126\n"
      "v_exp_f32 v127, v127\n"
      "v_smfmac_f32_32x32x32_bf16 v[78:93], v[30:33], v[118:125], v44\n"
      "v_exp_f32 v128, v128\n"
      "v_exp_f32 v129, v129\n"
      "v_exp_f32 v130, v130\n"
      "v_mfma_f32_32x32x16_bf16 v[110:125], v[22:25], v[10:13], v[46:61]\n"
      "v_exp_f32 v131, v131\n"
      "v_exp_f32 v132, v132\n"
      "v_exp_f32 v133, v133\n"
      "v_exp_f32 v134, v134\n"
      "v_exp_f32 v135, v135\n"
      "v_add_u32 v2, v3, v2\n"
      "v_smfmac_f32_32x32x32_bf16 v[94:109], v[26:29], v[126:133], v44\n"
      "v_exp_f32 v136, v136\n"
      "v_exp_f32 v137, v137\n"
      "v_exp_f32 v138, v138\n"
      "v_exp_f32 v139, v139\n"
      "v_exp_f32 v140, v140\n"
      "v_exp_f32 v141, v141\n"
      "s_waitcnt lgkmcnt(0)\n"
      "v_exp_f32 v110, v110\n"
      "v_exp_f32 v111, v111\n"
      "v_smfmac_f32_32x32x32_bf16 v[94:109], v[30:33], v[134:141], v44\n"
      "v_exp_f32 v112, v112\n"
      "v_exp_f32 v113, v113\n"
      "v_exp_f32 v114, v114\n"
      "v_mfma_f32_32x32x16_bf16 v[126:141], v[22:25], v[14:17], v[62:77]\n"
      "v_exp_f32 v115, v115\n"
      "v_exp_f32 v116, v116\n"
      "v_exp_f32 v117, v117\n"
      "v_exp_f32 v118, v118\n"
      "v_exp_f32 v119, v119\n"
      "v_smfmac_f32_32x32x32_bf16 v[78:93], v[34:37], v[110:117], v44\n"
      "v_exp_f32 v120, v120\n"
      "v_exp_f32 v121, v121\n"
      "v_exp_f32 v122, v122\n"
      "v_exp_f32 v123, v123\n"
      "v_exp_f32 v124, v124\n"
      "v_exp_f32 v125, v125\n"
      "v_readfirstlane_b32 s46, v8\n"
      "s_cmp_eq_u32 s46, 257\n"
      "s_cbranch_scc0 Lpm4_%=\n"
      "Lrm4_%=:\n"
      "ds_read_b64_tr_b16 v[26:27], v2 offset:0\n"
      "ds_read_b64_tr_b16 v[28:29], v2 offset:128\n"
      "ds_read_b64_tr_b16 v[30:31], v2 offset:512\n"
      "ds_read_b64_tr_b16 v[32:33], v2 offset:640\n"
      "ds_read_b128 v[22:25], v1 offset:9216\n"
      "v_exp_f32 v126, v126\n"
      "v_exp_f32 v127, v127\n"
      "v_smfmac_f32_32x32x32_bf16 v[78:93], v[38:41], v[118:125], v44\n"
      "v_exp_f32 v128, v128\n"
      "v_exp_f32 v129, v129\n"
      "v_exp_f32 v130, v130\n"
      "v_mfma_f32_32x32x16_bf16 v[110:125], v[18:21], v[10:13], v[46:61]\n"
      "v_exp_f32 v131, v131\n"
      "v_exp_f32 v132, v132\n"
      "v_exp_f32 v133, v133\n"
      "v_exp_f32 v134, v134\n"
      "v_exp_f32 v135, v135\n"
      "v_smfmac_f32_32x32x32_bf16 v[94:109], v[34:37], v[126:133], v44\n"
      "v_exp_f32 v136, v136\n"
      "v_exp_f32 v137, v137\n"
      "v_exp_f32 v138, v138\n"
      "v_exp_f32 v139, v139\n"
      "v_exp_f32 v140, v140\n"
      "v_exp_f32 v141, v141\n"
      "s_waitcnt lgkmcnt(0)\n"
      "v_exp_f32 v110, v110\n"
      "v_exp_f32 v111, v111\n"
      "v_smfmac_f32_32x32x32_bf16 v[94:109], v[38:41], v[134:141], v44\n"
      "v_exp_f32 v112, v112\n"
      "v_exp_f32 v113, v113\n"
      "v_exp_f32 v114, v114\n"
      "v_mfma_f32_32x32x16_bf16 v[126:141], v[18:21], v[14:17], v[62:77]\n"
      "v_exp_f32 v115, v115\n"
      "v_exp_f32 v116, v116\n"
      "v_exp_f32 v117, v117\n"
      "v_exp_f32 v118, v118\n"
      "v_exp_f32 v119, v119\n"
      "v_smfmac_f32_32x32x32_bf16 v[78:93], v[26:29], v[110:117], v44\n"
      "v_exp_f32 v120, v120\n"
      "v_exp_f32 v121, v121\n"
      "v_exp_f32 v122, v122\n"
      "v_exp_f32 v123, v123\n"
      "v_exp_f32 v124, v124\n"
      "v_exp_f32 v125, v125\n"
      "ds_read_b64_tr_b16 v[34:35], v2 offset:1024\n"
      "ds_read_b64_tr_b16 v[36:37], v2 offset:1152\n"
      "ds_read_b64_tr_b16 v[38:39], v2 offset:1536\n"
      "ds_read_b64_tr_b16 v[40:41], v2 offset:1664\n"
      "ds_read_b128 v[18:21], v1 offset:10240\n"
      "v_exp_f32 v126, v126\n"
      "v_exp_f32 v127, v127\n"
      "v_smfmac_f32_32x32x32_bf16 v[78:93], v[30:33], v[118:125], v44\n"
      "v_exp_f32 v128, v128\n"
      "v_exp_f32 v129, v129\n"
      "v_exp_f32 v130, v130\n"
      "v_mfma_f32_32x32x16_bf16 v[110:125], v[22:25], v[10:13], v[46:61]\n"
      "v_exp_f32 v131, v131\n"
      "v_exp_f32 v132, v132\n"
      "v_exp_f32 v133, v133\n"
      "v_exp_f32 v134, v134\n"
      "v_exp_f32 v135, v135\n"
      "v_smfmac_f32_32x32x32_bf16 v[94:109], v[26:29], v[126:133], v44\n"
      "v_exp_f32 v136, v136\n"
      "v_exp_f32 v137, v137\n"
      "v_exp_f32 v138, v138\n"
      "v_exp_f32 v139, v139\n"
      "v_exp_f32 v140, v140\n"
      "v_exp_f32 v141, v141\n"
      "s_waitcnt lgkmcnt(0)\n"
      "v_exp_f32 v110, v110\n"
      "v_exp_f32 v111, v111\n"
      "v_smfmac_f32_32x32x32_bf16 v[94:109], v[30:33], v[134:141], v44\n"
      "v_exp_f32 v112, v112\n"
      "v_exp_f32 v113, v113\n"
      "v_exp_f32 v114, v114\n"
      "v_mfma_f32_32x32x16_bf16 v[126:141], v[22:25], v[14:17], v[62:77]\n"
      "v_exp_f32 v115, v115\n"
      "v_exp_f32 v116, v116\n"
      "v_exp_f32 v117, v117\n"
      "v_exp_f32 v118, v118\n"
      "v_exp_f32 v119, v119\n"
      "v_smfmac_f32_32x32x32_bf16 v[78:93], v[34:37], v[110:117], v44\n"
      "v_exp_f32 v120, v120\n"
      "v_exp_f32 v121, v121\n"
      "v_exp_f32 v122, v122\n"
      "v_exp_f32 v123, v123\n"
      "v_exp_f32 v124, v124\n"
      "v_exp_f32 v125, v125\n"
      "ds_read_b64_tr_b16 v[26:27], v2 offset:2048\n"
      "ds_read_b64_tr_b16 v[28:29], v2 offset:2176\n"
      "ds_read_b64_tr_b16 v[30:31], v2 offset:2560\n"
      "ds_read_b64_tr_b16 v[32:33], v2 offset:2688\n"
      "ds_read_b128 v[22:25], v1 offset:11264\n"
      "v_exp_f32 v126, v126\n"
      "v_exp_f32 v127, v127\n"
      "v_smfmac_f32_32x32x32_bf16 v[78:93], v[38:41], v[118:125], v44\n"
      "v_exp_f32 v128, v128\n"
      "v_exp_f32 v129, v129\n"
      "v_exp_f32 v130, v130\n"
      "v_mfma_f32_32x32x16_bf16 v[110:125], v[18:21], v[10:13], v[46:61]\n"
      "v_exp_f32 v131, v131\n"
      "v_exp_f32 v132, v132\n"
      "v_exp_f32 v133, v133\n"
      "v_exp_f32 v134, v134\n"
      "v_exp_f32 v135, v135\n"
      "v_smfmac_f32_32x32x32_bf16 v[94:109], v[34:37], v[126:133], v44\n"
      "v_exp_f32 v136, v136\n"
      "v_exp_f32 v137, v137\n"
      "v_exp_f32 v138, v138\n"
      "v_exp_f32 v139, v139\n"
      "v_exp_f32 v140, v140\n"
      "v_exp_f32 v141, v141\n"
      "s_waitcnt lgkmcnt(0)\n"
      "v_exp_f32 v110, v110\n"
      "v_exp_f32 v111, v111\n"
      "v_smfmac_f32_32x32x32_bf16 v[94:109], v[38:41], v[134:141], v44\n"
      "v_exp_f32 v112, v112\n"
      "v_exp_f32 v113, v113\n"
      "v_exp_f32 v114, v114\n"
      "v_mfma_f32_32x32x16_bf16 v[126:141], v[18:21], v[14:17], v[62:77]\n"
      "v_exp_f32 v115, v115\n"
      "v_exp_f32 v116, v116\n"
      "v_exp_f32 v117, v117\n"
      "v_exp_f32 v118, v118\n"
      "v_exp_f32 v119, v119\n"
      "v_smfmac_f32_32x32x32_bf16 v[78:93], v[26:29], v[110:117], v44\n"
      "v_exp_f32 v120, v120\n"
      "v_exp_f32 v121, v121\n"
      "v_exp_f32 v122, v122\n"
      "v_exp_f32 v123, v123\n"
      "v_exp_f32 v124, v124\n"
      "v_exp_f32 v125, v125\n"
      "ds_read_b64_tr_b16 v[34:35], v2 offset:3072\n"
      "ds_read_b64_tr_b16 v[36:37], v2 offset:3200\n"
      "ds_read_b64_tr_b16 v[38:39], v2 offset:3584\n"
      "ds_read_b64_tr_b16 v[40:41], v2 offset:3712\n"
      "ds_read_b128 v[18:21], v1 offset:12288\n"
      "ds_read_b32 v8, v6 offset:20\n"
      "v_exp_f32 v126, v126\n"
      "v_exp_f32 v127, v127\n"
      "v_smfmac_f32_32x32x32_bf16 v[78:93], v[30:33], v[118:125], v44\n"
      "v_exp_f32 v128, v128\n"
      "v_exp_f32 v129, v129\n"
      "v_exp_f32 v130, v130\n"
      "v_mfma_f32_32x32x16_bf16 v[110:125], v[22:25], v[10:13], v[46:61]\n"
      "v_exp_f32 v131, v131\n"
      "v_exp_f32 v132, v132\n"
      "v_exp_f32 v133, v133\n"
      "v_exp_f32 v134, v134\n"
      "v_exp_f32 v135, v135\n"
      "v_smfmac_f32_32x32x32_bf16 v[94:109], v[26:29], v[126:133], v44\n"
      "v_exp_f32 v136, v136\n"
      "v_exp_f32 v137, v137\n"
      "v_exp_f32 v138, v138\n"
      "v_exp_f32 v139, v139\n"
      "v_exp_f32 v140, v140\n"
      "v_exp_f32 v141, v141\n"
      "s_waitcnt lgkmcnt(0)\n"
      "v_exp_f32 v110, v110\n"
      "v_exp_f32 v111, v111\n"
      "v_smfmac_f32_32x32x32_bf16 v[94:109], v[30:33], v[134:141], v44\n"
      "v_exp_f32 v112, v112\n"
      "v_exp_f32 v113, v113\n"
      "v_exp_f32 v114, v114\n"
      "v_mfma_f32_32x32x16_bf16 v[126:141], v[22:25], v[14:17], v[62:77]\n"
      "v_exp_f32 v115, v115\n"
      "v_exp_f32 v116, v116\n"
      "v_exp_f32 v117, v117\n"
      "v_exp_f32 v118, v118\n"
      "v_exp_f32 v119, v119\n"
      "v_smfmac_f32_32x32x32_bf16 v[78:93], v[34:37], v[110:117], v44\n"
      "v_exp_f32 v120, v120\n"
      "v_exp_f32 v121, v121\n"
      "v_exp_f32 v122, v122\n"
      "v_exp_f32 v123, v123\n"
      "v_exp_f32 v124, v124\n"
      "v_exp_f32 v125, v125\n"
      "v_readfirstlane_b32 s46, v8\n"
      "s_cmp_eq_u32 s46, 257\n"
      "s_cbranch_scc0 Lpm5_%=\n"
      "Lrm5_%=:\n"
      "ds_read_b64_tr_b16 v[26:27], v2 offset:4096\n"
      "ds_read_b64_tr_b16 v[28:29], v2 offset:4224\n"
      "ds_read_b64_tr_b16 v[30:31], v2 offset:4608\n"
      "ds_read_b64_tr_b16 v[32:33], v2 offset:4736\n"
      "ds_read_b128 v[22:25], v1 offset:13312\n"
      "v_exp_f32 v126, v126\n"
      "v_exp_f32 v127, v127\n"
      "v_smfmac_f32_32x32x32_bf16 v[78:93], v[38:41], v[118:125], v44\n"
      "v_exp_f32 v128, v128\n"
      "v_exp_f32 v129, v129\n"
      "v_exp_f32 v130, v130\n"
      "v_mfma_f32_32x32x16_bf16 v[110:125], v[18:21], v[10:13], v[46:61]\n"
      "v_exp_f32 v131, v131\n"
      "v_exp_f32 v132, v132\n"
      "v_exp_f32 v133, v133\n"
      "v_exp_f32 v134, v134\n"
      "v_exp_f32 v135, v135\n"
      "v_smfmac_f32_32x32x32_bf16 v[94:109], v[34:37], v[126:133], v44\n"
      "v_exp_f32 v136, v136\n"
      "v_exp_f32 v137, v137\n"
      "v_exp_f32 v138, v138\n"
      "v_exp_f32 v139, v139\n"
      "v_exp_f32 v140, v140\n"
      "v_exp_f32 v141, v141\n"
      "s_waitcnt lgkmcnt(0)\n"
      "v_exp_f32 v110, v110\n"
      "v_exp_f32 v111, v111\n"
      "v_smfmac_f32_32x32x32_bf16 v[94:109], v[38:41], v[134:141], v44\n"
      "v_exp_f32 v112, v112\n"
      "v_exp_f32 v113, v113\n"
      "v_exp_f32 v114, v114\n"
      "v_mfma_f32_32x32x16_bf16 v[126:141], v[18:21], v[14:17], v[62:77]\n"
      "v_exp_f32 v115, v115\n"
      "v_exp_f32 v116, v116\n"
      "v_exp_f32 v117, v117\n"
      "v_exp_f32 v118, v118\n"
      "v_exp_f32 v119, v119\n"
      "v_smfmac_f32_32x32x32_bf16 v[78:93], v[26:29], v[110:117], v44\n"
      "v_exp_f32 v120, v120\n"
      "v_exp_f32 v121, v121\n"
      "v_exp_f32 v122, v122\n"
      "v_exp_f32 v123, v123\n"
      "v_exp_f32 v124, v124\n"
      "v_exp_f32 v125, v125\n"
      "ds_read_b64_tr_b16 v[34:35], v2 offset:5120\n"
      "ds_read_b64_tr_b16 v[36:37], v2 offset:5248\n"
      "ds_read_b64_tr_b16 v[38:39], v2 offset:5632\n"
      "ds_read_b64_tr_b16 v[40:41], v2 offset:5760\n"
      "ds_read_b128 v[18:21], v1 offset:14336\n"
      "v_exp_f32 v126, v126\n"
      "v_exp_f32 v127, v127\n"
      "v_smfmac_f32_32x32x32_bf16 v[78:93], v[30:33], v[118:125], v44\n"
      "v_exp_f32 v128, v128\n"
      "v_exp_f32 v129, v129\n"
      "v_exp_f32 v130, v130\n"
      "v_mfma_f32_32x32x16_bf16 v[110:125], v[22:25], v[10:13], v[46:61]\n"
      "v_exp_f32 v131, v131\n"
      "v_exp_f32 v132, v132\n"
      "v_exp_f32 v133, v133\n"
      "v_exp_f32 v134, v134\n"
      "v_exp_f32 v135, v135\n"
      "v_smfmac_f32_32x32x32_bf16 v[94:109], v[26:29], v[126:133], v44\n"
      "v_exp_f32 v136, v136\n"
      "v_exp_f32 v137, v137\n"
      "v_exp_f32 v138, v138\n"
      "v_exp_f32 v139, v139\n"
      "v_exp_f32 v140, v140\n"
      "v_exp_f32 v141, v141\n"
      "s_waitcnt lgkmcnt(0)\n"
      "v_exp_f32 v110, v110\n"
      "v_exp_f32 v111, v111\n"
      "v_smfmac_f32_32x32x32_bf16 v[94:109], v[30:33], v[134:141], v44\n"
      "v_exp_f32 v112, v112\n"
      "v_exp_f32 v113, v113\n"
      "v_exp_f32 v114, v114\n"
      "v_mfma_f32_32x32x16_bf16 v[126:141], v[22:25], v[14:17], v[62:77]\n"
      "v_exp_f32 v115, v115\n"
      "v_exp_f32 v116, v116\n"
      "v_exp_f32 v117, v117\n"
      "v_exp_f32 v118, v118\n"
      "v_exp_f32 v119, v119\n"
      "v_smfmac_f32_32x32x32_bf16 v[78:93], v[34:37], v[110:117], v44\n"
      "v_exp_f32 v120, v120\n"
      "v_exp_f32 v121, v121\n"
      "v_exp_f32 v122, v122\n"
      "v_exp_f32 v123, v123\n"
      "v_exp_f32 v124, v124\n"
      "v_exp_f32 v125, v125\n"
      "ds_read_b64_tr_b16 v[26:27], v2 offset:6144\n"
      "ds_read_b64_tr_b16 v[28:29], v2 offset:6272\n"
      "ds_read_b64_tr_b16 v[30:31], v2 offset:6656\n"
      "ds_read_b64_tr_b16 v[32:33], v2 offset:6784\n"
      "ds_read_b128 v[22:25], v1 offset:15360\n"
      "v_exp_f32 v126, v126\n"
      "v_exp_f32 v127, v127\n"
      "v_smfmac_f32_32x32x32_bf16 v[78:93], v[38:41], v[118:125], v44\n"
      "v_exp_f32 v128, v128\n"
      "v_exp_f32 v129, v129\n"
      "v_exp_f32 v130, v130\n"
      "v_mfma_f32_32x32x16_bf16 v[110:125], v[18:21], v[10:13], v[46:61]\n"
      "v_exp_f32 v131, v131\n"
      "v_exp_f32 v132, v132\n"
      "v_exp_f32 v133, v133\n"
      "v_exp_f32 v134, v134\n"
      "v_exp_f32 v135, v135\n"
      "v_smfmac_f32_32x32x32_bf16 v[94:109], v[34:37], v[126:133], v44\n"
      "v_exp_f32 v136, v136\n"
      "v_exp_f32 v137, v137\n"
      "v_exp_f32 v138, v138\n"
      "v_exp_f32 v139, v139\n"
      "v_exp_f32 v140, v140\n"
      "v_exp_f32 v141, v141\n"
      "s_waitcnt lgkmcnt(0)\n"
      "v_exp_f32 v110, v110\n"
      "v_exp_f32 v111, v111\n"
      "v_smfmac_f32_32x32x32_bf16 v[94:109], v[38:41], v[134:141], v44\n"
      "v_exp_f32 v112, v112\n"
      "v_exp_f32 v113, v113\n"
      "v_exp_f32 v114, v114\n"
      "v_mfma_f32_32x32x16_bf16 v[126:141], v[18:21], v[14:17], v[62:77]\n"
      "v_exp_f32 v115, v115\n"
      "v_exp_f32 v116, v116\n"
      "v_exp_f32 v117, v117\n"
      "v_exp_f32 v118, v118\n"
      "v_exp_f32 v119, v119\n"
      "v_smfmac_f32_32x32x32_bf16 v[78:93], v[26:29], v[110:117], v44\n"
      "v_exp_f32 v120, v120\n"
      "v_exp_f32 v121, v121\n"
      "v_exp_f32 v122, v122\n"
      "v_exp_f32 v123, v123\n"
      "v_exp_f32 v124, v124\n"
      "v_exp_f32 v125, v125\n"
      "ds_read_b64_tr_b16 v[34:35], v2 offset:7168\n"
      "ds_read_b64_tr_b16 v[36:37], v2 offset:7296\n"
      "ds_read_b64_tr_b16 v[38:39], v2 offset:7680\n"
      "ds_read_b64_tr_b16 v[40:41], v2 offset:7808\n"
      "ds_read_b128 v[18:21], v1 offset:16384\n"
      "v_exp_f32 v126, v126\n"
      "v_exp_f32 v127, v127\n"
      "v_smfmac_f32_32x32x32_bf16 v[78:93], v[30:33], v[118:125], v44\n"
      "v_exp_f32 v128, v128\n"
      "v_exp_f32 v129, v129\n"
      "v_exp_f32 v130, v130\n"
      "v_mfma_f32_32x32x16_bf16 v[110:125], v[22:25], v[10:13], v[46:61]\n"
      "v_exp_f32 v131, v131\n"
      "v_exp_f32 v132, v132\n"
      "v_exp_f32 v133, v133\n"
      "v_exp_f32 v134, v134\n"
      "v_exp_f32 v135, v135\n"
      "v_add_u32 v2, v3, v2\n"
      "v_smfmac_f32_32x32x32_bf16 v[94:109], v[26:29], v[126:133], v44\n"
      "v_exp_f32 v136, v136\n"
      "v_exp_f32 v137, v137\n"
      "v_exp_f32 v138, v138\n"
      "v_exp_f32 v139, v139\n"
      "v_exp_f32 v140, v140\n"
      "v_exp_f32 v141, v141\n"
      "s_waitcnt lgkmcnt(0)\n"
      "v_exp_f32 v110, v110\n"
      "v_exp_f32 v111, v111\n"
      "v_smfmac_f32_32x32x32_bf16 v[94:109], v[30:33], v[134:141], v44\n"
      "v_exp_f32 v112, v112\n"
      "v_exp_f32 v113, v113\n"
      "v_exp_f32 v114, v114\n"
      "v_mfma_f32_32x32x16_bf16 v[126:141], v[22:25], v[14:17], v[62:77]\n"
      "v_exp_f32 v115, v115\n"
      "v_exp_f32 v116, v116\n"
      "v_exp_f32 v117, v117\n"
      "v_exp_f32 v118, v118\n"
      "v_exp_f32 v119, v119\n"
      "v_smfmac_f32_32x32x32_bf16 v[78:93], v[34:37], v[110:117], v44\n"
      "v_exp_f32 v120, v120\n"
      "v_exp_f32 v121, v121\n"
      "v_exp_f32 v122, v122\n"
      "v_exp_f32 v123, v123\n"
      "v_exp_f32 v124, v124\n"
      "v_exp_f32 v125, v125\n"
      "ds_read_b64_tr_b16 v[26:27], v2 offset:0\n"
      "ds_read_b64_tr_b16 v[28:29], v2 offset:128\n"
      "ds_read_b64_tr_b16 v[30:31], v2 offset:512\n"
      "ds_read_b64_tr_b16 v[32:33], v2 offset:640\n"
      "ds_read_b128 v[22:25], v1 offset:17408\n"
      "v_exp_f32 v126, v126\n"
      "v_exp_f32 v127, v127\n"
      "v_smfmac_f32_32x32x32_bf16 v[78:93], v[38:41], v[118:125], v44\n"
      "v_exp_f32 v128, v128\n"
      "v_exp_f32 v129, v129\n"
      "v_exp_f32 v130, v130\n"
      "v_mfma_f32_32x32x16_bf16 v[110:125], v[18:21], v[10:13], v[46:61]\n"
      "v_exp_f32 v131, v131\n"
      "v_exp_f32 v132, v132\n"
      "v_exp_f32 v133, v133\n"
      "v_exp_f32 v134, v134\n"
      "v_exp_f32 v135, v135\n"
      "v_smfmac_f32_32x32x32_bf16 v[94:109], v[34:37], v[126:133], v44\n"
      "v_exp_f32 v136, v136\n"
      "v_exp_f32 v137, v137\n"
      "v_exp_f32 v138, v138\n"
      "v_exp_f32 v139, v139\n"
      "v_exp_f32 v140, v140\n"
      "v_exp_f32 v141, v141\n"
      "s_waitcnt lgkmcnt(0)\n"
      "v_exp_f32 v110, v110\n"
      "v_exp_f32 v111, v111\n"
      "v_smfmac_f32_32x32x32_bf16 v[94:109], v[38:41], v[134:141], v44\n"
      "v_exp_f32 v112, v112\n"
      "v_exp_f32 v113, v113\n"
      "v_exp_f32 v114, v114\n"
      "v_mfma_f32_32x32x16_bf16 v[126:141], v[18:21], v[14:17], v[62:77]\n"
      "v_exp_f32 v115, v115\n"
      "v_exp_f32 v116, v116\n"
      "v_exp_f32 v117, v117\n"
      "v_exp_f32 v118, v118\n"
      "v_exp_f32 v119, v119\n"
      "v_smfmac_f32_32x32x32_bf16 v[78:93], v[26:29], v[110:117], v44\n"
      "v_exp_f32 v120, v120\n"
      "v_exp_f32 v121, v121\n"
      "v_exp_f32 v122, v122\n"
      "v_exp_f32 v123, v123\n"
      "v_exp_f32 v124, v124\n"
      "v_exp_f32 v125, v125\n"
      "ds_read_b64_tr_b16 v[34:35], v2 offset:1024\n"
      "ds_read_b64_tr_b16 v[36:37], v2 offset:1152\n"
      "ds_read_b64_tr_b16 v[38:39], v2 offset:1536\n"
      "ds_read_b64_tr_b16 v[40:41], v2 offset:1664\n"
      "ds_read_b128 v[18:21], v1 offset:18432\n"
      "ds_read_b32 v8, v6 offset:24\n"
      "v_exp_f32 v126, v126\n"
      "v_exp_f32 v127, v127\n"
      "v_smfmac_f32_32x32x32_bf16 v[78:93], v[30:33], v[118:125], v44\n"
      "v_exp_f32 v128, v128\n"
      "v_exp_f32 v129, v129\n"
      "v_exp_f32 v130, v130\n"
      "v_mfma_f32_32x32x16_bf16 v[110:125], v[22:25], v[10:13], v[46:61]\n"
      "v_exp_f32 v131, v131\n"
      "v_exp_f32 v132, v132\n"
      "v_exp_f32 v133, v133\n"
      "v_exp_f32 v134, v134\n"
      "v_exp_f32 v135, v135\n"
      "v_smfmac_f32_32x32x32_bf16 v[94:109], v[26:29], v[126:133], v44\n"
      "v_exp_f32 v136, v136\n"
      "v_exp_f32 v137, v137\n"
      "v_exp_f32 v138, v138\n"
      "v_exp_f32 v139, v139\n"
      "v_exp_f32 v140, v140\n"
      "v_exp_f32 v141, v141\n"
      "s_waitcnt lgkmcnt(0)\n"
      "v_exp_f32 v110, v110\n"
      "v_exp_f32 v111, v111\n"
      "v_smfmac_f32_32x32x32_bf16 v[94:109], v[30:33], v[134:141], v44\n"
      "v_exp_f32 v112, v112\n"
      "v_exp_f32 v113, v113\n"
      "v_exp_f32 v114, v114\n"
      "v_mfma_f32_32x32x16_bf16 v[126:141], v[22:25], v[14:17], v[62:77]\n"
      "v_exp_f32 v115, v115\n"
      "v_exp_f32 v116, v116\n"
      "v_exp_f32 v117, v117\n"
      "v_exp_f32 v118, v118\n"
      "v_exp_f32 v119, v119\n"
      "v_smfmac_f32_32x32x32_bf16 v[78:93], v[34:37], v[110:117], v44\n"
      "v_exp_f32 v120, v120\n"
      "v_exp_f32 v121, v121\n"
      "v_exp_f32 v122, v122\n"
      "v_exp_f32 v123, v123\n"
      "v_exp_f32 v124, v124\n"
      "v_exp_f32 v125, v125\n"
      "v_readfirstlane_b32 s46, v8\n"
      "s_cmp_eq_u32 s46, 257\n"
      "s_cbranch_scc0 Lpm6_%=\n"
      "Lrm6_%=:\n"
      "ds_read_b64_tr_b16 v[26:27], v2 offset:2048\n"
      "ds_read_b64_tr_b16 v[28:29], v2 offset:2176\n"
      "ds_read_b64_tr_b16 v[30:31], v2 offset:2560\n"
      "ds_read_b64_tr_b16 v[32:33], v2 offset:2688\n"
      "ds_read_b128 v[22:25], v1 offset:19456\n"
      "v_exp_f32 v126, v126\n"
      "v_exp_f32 v127, v127\n"
      "v_smfmac_f32_32x32x32_bf16 v[78:93], v[38:41], v[118:125], v44\n"
      "v_exp_f32 v128, v128\n"
      "v_exp_f32 v129, v129\n"
      "v_exp_f32 v130, v130\n"
      "v_mfma_f32_32x32x16_bf16 v[110:125], v[18:21], v[10:13], v[46:61]\n"
      "v_exp_f32 v131, v131\n"
      "v_exp_f32 v132, v132\n"
      "v_exp_f32 v133, v133\n"
      "v_exp_f32 v134, v134\n"
      "v_exp_f32 v135, v135\n"
      "v_smfmac_f32_32x32x32_bf16 v[94:109], v[34:37], v[126:133], v44\n"
      "v_exp_f32 v136, v136\n"
      "v_exp_f32 v137, v137\n"
      "v_exp_f32 v138, v138\n"
      "v_exp_f32 v139, v139\n"
      "v_exp_f32 v140, v140\n"
      "v_exp_f32 v141, v141\n"
      "s_waitcnt lgkmcnt(0)\n"
      "v_exp_f32 v110, v110\n"
      "v_exp_f32 v111, v111\n"
      "v_smfmac_f32_32x32x32_bf16 v[94:109], v[38:41], v[134:141], v44\n"
      "v_exp_f32 v112, v112\n"
      "v_exp_f32 v113, v113\n"
      "v_exp_f32 v114, v114\n"
      "v_mfma_f32_32x32x16_bf16 v[126:141], v[18:21], v[14:17], v[62:77]\n"
      "v_exp_f32 v115, v115\n"
      "v_exp_f32 v116, v116\n"
      "v_exp_f32 v117, v117\n"
      "v_exp_f32 v118, v118\n"
      "v_exp_f32 v119, v119\n"
      "v_smfmac_f32_32x32x32_bf16 v[78:93], v[26:29], v[110:117], v44\n"
      "v_exp_f32 v120, v120\n"
      "v_exp_f32 v121, v121\n"
      "v_exp_f32 v122, v122\n"
      "v_exp_f32 v123, v123\n"
      "v_exp_f32 v124, v124\n"
      "v_exp_f32 v125, v125\n"
      "ds_read_b64_tr_b16 v[34:35], v2 offset:3072\n"
      "ds_read_b64_tr_b16 v[36:37], v2 offset:3200\n"
      "ds_read_b64_tr_b16 v[38:39], v2 offset:3584\n"
      "ds_read_b64_tr_b16 v[40:41], v2 offset:3712\n"
      "ds_read_b128 v[18:21], v1 offset:20480\n"
      "v_exp_f32 v126, v126\n"
      "v_exp_f32 v127, v127\n"
      "v_smfmac_f32_32x32x32_bf16 v[78:93], v[30:33], v[118:125], v44\n"
      "v_exp_f32 v128, v128\n"
      "v_exp_f32 v129, v129\n"
      "v_exp_f32 v130, v130\n"
      "v_mfma_f32_32x32x16_bf16 v[110:125], v[22:25], v[10:13], v[46:61]\n"
      "v_exp_f32 v131, v131\n"
      "v_exp_f32 v132, v132\n"
      "v_exp_f32 v133, v133\n"
      "v_exp_f32 v134, v134\n"
      "v_exp_f32 v135, v135\n"
      "v_smfmac_f32_32x32x32_bf16 v[94:109], v[26:29], v[126:133], v44\n"
      "v_exp_f32 v136, v136\n"
      "v_exp_f32 v137, v137\n"
      "v_exp_f32 v138, v138\n"
      "v_exp_f32 v139, v139\n"
      "v_exp_f32 v140, v140\n"
      "v_exp_f32 v141, v141\n"
      "s_waitcnt lgkmcnt(0)\n"
      "v_exp_f32 v110, v110\n"
      "v_exp_f32 v111, v111\n"
      "v_smfmac_f32_32x32x32_bf16 v[94:109], v[30:33], v[134:141], v44\n"
      "v_exp_f32 v112, v112\n"
      "v_exp_f32 v113, v113\n"
      "v_exp_f32 v114, v114\n"
      "v_mfma_f32_32x32x16_bf16 v[126:141], v[22:25], v[14:17], v[62:77]\n"
      "v_exp_f32 v115, v115\n"
      "v_exp_f32 v116, v116\n"
      "v_exp_f32 v117, v117\n"
      "v_exp_f32 v118, v118\n"
      "v_exp_f32 v119, v119\n"
      "v_smfmac_f32_32x32x32_bf16 v[78:93], v[34:37], v[110:117], v44\n"
      "v_exp_f32 v120, v120\n"
      "v_exp_f32 v121, v121\n"
      "v_exp_f32 v122, v122\n"
      "v_exp_f32 v123, v123\n"
      "v_exp_f32 v124, v124\n"
      "v_exp_f32 v125, v125\n"
      "ds_read_b64_tr_b16 v[26:27], v2 offset:4096\n"
      "ds_read_b64_tr_b16 v[28:29], v2 offset:4224\n"
      "ds_read_b64_tr_b16 v[30:31], v2 offset:4608\n"
      "ds_read_b64_tr_b16 v[32:33], v2 offset:4736\n"
      "ds_read_b128 v[22:25], v1 offset:21504\n"
      "v_exp_f32 v126, v126\n"
      "v_exp_f32 v127, v127\n"
      "v_smfmac_f32_32x32x32_bf16 v[78:93], v[38:41], v[118:125], v44\n"
      "v_exp_f32 v128, v128\n"
      "v_exp_f32 v129, v129\n"
      "v_exp_f32 v130, v130\n"
      "v_mfma_f32_32x32x16_bf16 v[110:125], v[18:21], v[10:13], v[46:61]\n"
      "v_exp_f32 v131, v131\n"
      "v_exp_f32 v132, v132\n"
      "v_exp_f32 v133, v133\n"
      "v_exp_f32 v134, v134\n"
      "v_exp_f32 v135, v135\n"
      "v_smfmac_f32_32x32x32_bf16 v[94:109], v[34:37], v[126:133], v44\n"
      "v_exp_f32 v136, v136\n"
      "v_exp_f32 v137, v137\n"
      "v_exp_f32 v138, v138\n"
      "v_exp_f32 v139, v139\n"
      "v_exp_f32 v140, v140\n"
      "v_exp_f32 v141, v141\n"
      "s_waitcnt lgkmcnt(0)\n"
      "v_exp_f32 v110, v110\n"
      "v_exp_f32 v111, v111\n"
      "v_smfmac_f32_32x32x32_bf16 v[94:109], v[38:41], v[134:141], v44\n"
      "v_exp_f32 v112, v112\n"
      "v_exp_f32 v113, v113\n"
      "v_exp_f32 v114, v114\n"
      "v_mfma_f32_32x32x16_bf16 v[126:141], v[18:21], v[14:17], v[62:77]\n"
      "v_exp_f32 v115, v115\n"
      "v_exp_f32 v116, v116\n"
      "v_exp_f32 v117, v117\n"
      "v_exp_f32 v118, v118\n"
      "v_exp_f32 v119, v119\n"
      "v_smfmac_f32_32x32x32_bf16 v[78:93], v[26:29], v[110:117], v44\n"
      "v_exp_f32 v120, v120\n"
      "v_exp_f32 v121, v121\n"
      "v_exp_f32 v122, v122\n"
      "v_exp_f32 v123, v123\n"
      "v_exp_f32 v124, v124\n"
      "v_exp_f32 v125, v125\n"
      "ds_read_b64_tr_b16 v[34:35], v2 offset:5120\n"
      "ds_read_b64_tr_b16 v[36:37], v2 offset:5248\n"
      "ds_read_b64_tr_b16 v[38:39], v2 offset:5632\n"
      "ds_read_b64_tr_b16 v[40:41], v2 offset:5760\n"
      "ds_read_b128 v[18:21], v1 offset:22528\n"
      "v_exp_f32 v126, v126\n"
      "v_exp_f32 v127, v127\n"
      "v_smfmac_f32_32x32x32_bf16 v[78:93], v[30:33], v[118:125], v44\n"
      "v_exp_f32 v128, v128\n"
      "v_exp_f32 v129, v129\n"
      "v_exp_f32 v130, v130\n"
      "v_mfma_f32_32x32x16_bf16 v[110:125], v[22:25], v[10:13], v[46:61]\n"
      "v_exp_f32 v131, v131\n"
      "v_exp_f32 v132, v132\n"
      "v_exp_f32 v133, v133\n"
      "v_exp_f32 v134, v134\n"
      "v_exp_f32 v135, v135\n"
      "v_smfmac_f32_32x32x32_bf16 v[94:109], v[26:29], v[126:133], v44\n"
      "v_exp_f32 v136, v136\n"
      "v_exp_f32 v137, v137\n"
      "v_exp_f32 v138, v138\n"
      "v_exp_f32 v139, v139\n"
      "v_exp_f32 v140, v140\n"
      "v_exp_f32 v141, v141\n"
      "s_waitcnt lgkmcnt(0)\n"
      "v_exp_f32 v110, v110\n"
      "v_exp_f32 v111, v111\n"
      "v_smfmac_f32_32x32x32_bf16 v[94:109], v[30:33], v[134:141], v44\n"
      "v_exp_f32 v112, v112\n"
      "v_exp_f32 v113, v113\n"
      "v_exp_f32 v114, v114\n"
      "v_mfma_f32_32x32x16_bf16 v[126:141], v[22:25], v[14:17], v[62:77]\n"
      "v_exp_f32 v115, v115\n"
      "v_exp_f32 v116, v116\n"
      "v_exp_f32 v117, v117\n"
      "v_exp_f32 v118, v118\n"
      "v_exp_f32 v119, v119\n"
      "v_smfmac_f32_32x32x32_bf16 v[78:93], v[34:37], v[110:117], v44\n"
      "v_exp_f32 v120, v120\n"
      "v_exp_f32 v121, v121\n"
      "v_exp_f32 v122, v122\n"
      "v_exp_f32 v123, v123\n"
      "v_exp_f32 v124, v124\n"
      "v_exp_f32 v125, v125\n"
      "ds_read_b64_tr_b16 v[26:27], v2 offset:6144\n"
      "ds_read_b64_tr_b16 v[28:29], v2 offset:6272\n"
      "ds_read_b64_tr_b16 v[30:31], v2 offset:6656\n"
      "ds_read_b64_tr_b16 v[32:33], v2 offset:6784\n"
      "ds_read_b128 v[22:25], v1 offset:23552\n"
      "v_exp_f32 v126, v126\n"
      "v_exp_f32 v127, v127\n"
      "v_smfmac_f32_32x32x32_bf16 v[78:93], v[38:41], v[118:125], v44\n"
      "v_exp_f32 v128, v128\n"
      "v_exp_f32 v129, v129\n"
      "v_exp_f32 v130, v130\n"
      "v_mfma_f32_32x32x16_bf16 v[110:125], v[18:21], v[10:13], v[46:61]\n"
      "v_exp_f32 v131, v131\n"
      "v_exp_f32 v132, v132\n"
      "v_exp_f32 v133, v133\n"
      "v_exp_f32 v134, v134\n"
      "v_exp_f32 v135, v135\n"
      "v_smfmac_f32_32x32x32_bf16 v[94:109], v[34:37], v[126:133], v44\n"
      "v_exp_f32 v136, v136\n"
      "v_exp_f32 v137, v137\n"
      "v_exp_f32 v138, v138\n"
      "v_exp_f32 v139, v139\n"
      "v_exp_f32 v140, v140\n"
      "v_exp_f32 v141, v141\n"
      "s_waitcnt lgkmcnt(0)\n"
      "v_exp_f32 v110, v110\n"
      "v_exp_f32 v111, v111\n"
      "v_smfmac_f32_32x32x32_bf16 v[94:109], v[38:41], v[134:141], v44\n"
      "v_exp_f32 v112, v112\n"
      "v_exp_f32 v113, v113\n"
      "v_exp_f32 v114, v114\n"
      "v_mfma_f32_32x32x16_bf16 v[126:141], v[18:21], v[14:17], v[62:77]\n"
      "v_exp_f32 v115, v115\n"
      "v_exp_f32 v116, v116\n"
      "v_exp_f32 v117, v117\n"
      "v_exp_f32 v118, v118\n"
      "v_exp_f32 v119, v119\n"
      "v_smfmac_f32_32x32x32_bf16 v[78:93], v[26:29], v[110:117], v44\n"
      "v_exp_f32 v120, v120\n"
      "v_exp_f32 v121, v121\n"
      "v_exp_f32 v122, v122\n"
      "v_exp_f32 v123, v123\n"
      "v_exp_f32 v124, v124\n"
      "v_exp_f32 v125, v125\n"
      "ds_read_b64_tr_b16 v[34:35], v2 offset:7168\n"
      "ds_read_b64_tr_b16 v[36:37], v2 offset:7296\n"
      "ds_read_b64_tr_b16 v[38:39], v2 offset:7680\n"
      "ds_read_b64_tr_b16 v[40:41], v2 offset:7808\n"
      "ds_read_b128 v[18:21], v1 offset:24576\n"
      "v_exp_f32 v126, v126\n"
      "v_exp_f32 v127, v127\n"
      "v_smfmac_f32_32x32x32_bf16 v[78:93], v[30:33], v[118:125], v44\n"
      "v_exp_f32 v128, v128\n"
      "v_exp_f32 v129, v129\n"
      "v_exp_f32 v130, v130\n"
      "v_mfma_f32_32x32x16_bf16 v[110:125], v[22:25], v[10:13], v[46:61]\n"
      "v_exp_f32 v131, v131\n"
      "v_exp_f32 v132, v132\n"
      "v_exp_f32 v133, v133\n"
      "v_exp_f32 v134, v134\n"
      "v_exp_f32 v135, v135\n"
      "v_add_u32 v2, v3, v2\n"
      "v_smfmac_f32_32x32x32_bf16 v[94:109], v[26:29], v[126:133], v44\n"
      "v_exp_f32 v136, v136\n"
      "v_exp_f32 v137, v137\n"
      "v_exp_f32 v138, v138\n"
      "v_exp_f32 v139, v139\n"
      "v_exp_f32 v140, v140\n"
      "v_exp_f32 v141, v141\n"
      "s_waitcnt lgkmcnt(0)\n"
      "v_exp_f32 v110, v110\n"
      "v_exp_f32 v111, v111\n"
      "v_smfmac_f32_32x32x32_bf16 v[94:109], v[30:33], v[134:141], v44\n"
      "v_exp_f32 v112, v112\n"
      "v_exp_f32 v113, v113\n"
      "v_exp_f32 v114, v114\n"
      "v_mfma_f32_32x32x16_bf16 v[126:141], v[22:25], v[14:17], v[62:77]\n"
      "v_exp_f32 v115, v115\n"
      "v_exp_f32 v116, v116\n"
      "v_exp_f32 v117, v117\n"
      "v_exp_f32 v118, v118\n"
      "v_exp_f32 v119, v119\n"
      "v_smfmac_f32_32x32x32_bf16 v[78:93], v[34:37], v[110:117], v44\n"
      "v_exp_f32 v120, v120\n"
      "v_exp_f32 v121, v121\n"
      "v_exp_f32 v122, v122\n"
      "v_exp_f32 v123, v123\n"
      "v_exp_f32 v124, v124\n"
      "v_exp_f32 v125, v125\n"
      "ds_read_b64_tr_b16 v[26:27], v2 offset:0\n"
      "ds_read_b64_tr_b16 v[28:29], v2 offset:128\n"
      "ds_read_b64_tr_b16 v[30:31], v2 offset:512\n"
      "ds_read_b64_tr_b16 v[32:33], v2 offset:640\n"
      "ds_read_b128 v[22:25], v1 offset:25600\n"
      "ds_read_b32 v8, v6 offset:28\n"
      "v_exp_f32 v126, v126\n"
      "v_exp_f32 v127, v127\n"
      "v_smfmac_f32_32x32x32_bf16 v[78:93], v[38:41], v[118:125], v44\n"
      "v_exp_f32 v128, v128\n"
      "v_exp_f32 v129, v129\n"
      "v_exp_f32 v130, v130\n"
      "v_mfma_f32_32x32x16_bf16 v[110:125], v[18:21], v[10:13], v[46:61]\n"
      "v_exp_f32 v131, v131\n"
      "v_exp_f32 v132, v132\n"
      "v_exp_f32 v133, v133\n"
      "v_exp_f32 v134, v134\n"
      "v_exp_f32 v135, v135\n"
      "v_smfmac_f32_32x32x32_bf16 v[94:109], v[34:37], v[126:133], v44\n"
      "v_exp_f32 v136, v136\n"
      "v_exp_f32 v137, v137\n"
      "v_exp_f32 v138, v138\n"
      "v_exp_f32 v139, v139\n"
      "v_exp_f32 v140, v140\n"
      "v_exp_f32 v141, v141\n"
      "s_waitcnt lgkmcnt(0)\n"
      "v_exp_f32 v110, v110\n"
      "v_exp_f32 v111, v111\n"
      "v_smfmac_f32_32x32x32_bf16 v[94:109], v[38:41], v[134:141], v44\n"
      "v_exp_f32 v112, v112\n"
      "v_exp_f32 v113, v113\n"
      "v_exp_f32 v114, v114\n"
      "v_mfma_f32_32x32x16_bf16 v[126:141], v[18:21], v[14:17], v[62:77]\n"
      "v_exp_f32 v115, v115\n"
      "v_exp_f32 v116, v116\n"
      "v_exp_f32 v117, v117\n"
      "v_exp_f32 v118, v118\n"
      "v_exp_f32 v119, v119\n"
      "v_smfmac_f32_32x32x32_bf16 v[78:93], v[26:29], v[110:117], v44\n"
      "v_exp_f32 v120, v120\n"
      "v_exp_f32 v121, v121\n"
      "v_exp_f32 v122, v122\n"
      "v_exp_f32 v123, v123\n"
      "v_exp_f32 v124, v124\n"
      "v_exp_f32 v125, v125\n"
      "v_readfirstlane_b32 s46, v8\n"
      "s_cmp_eq_u32 s46, 257\n"
      "s_cbranch_scc0 Lpm7_%=\n"
      "Lrm7_%=:\n"
      "ds_read_b64_tr_b16 v[34:35], v2 offset:1024\n"
      "ds_read_b64_tr_b16 v[36:37], v2 offset:1152\n"
      "ds_read_b64_tr_b16 v[38:39], v2 offset:1536\n"
      "ds_read_b64_tr_b16 v[40:41], v2 offset:1664\n"
      "ds_read_b128 v[18:21], v1 offset:26624\n"
      "s_setprio 2\n"
      "v_exp_f32 v126, v126\n"
      "v_exp_f32 v127, v127\n"
      "v_smfmac_f32_32x32x32_bf16 v[78:93], v[30:33], v[118:125], v44\n"
      "v_exp_f32 v128, v128\n"
      "v_exp_f32 v129, v129\n"
      "v_exp_f32 v130, v130\n"
      "v_mfma_f32_32x32x16_bf16 v[110:125], v[22:25], v[10:13], v[46:61]\n"
      "v_exp_f32 v131, v131\n"
      "v_exp_f32 v132, v132\n"
      "v_exp_f32 v133, v133\n"
      "v_exp_f32 v134, v134\n"
      "v_exp_f32 v135, v135\n"
      "v_smfmac_f32_32x32x32_bf16 v[94:109], v[26:29], v[126:133], v44\n"
      "v_exp_f32 v136, v136\n"
      "v_exp_f32 v137, v137\n"
      "v_exp_f32 v138, v138\n"
      "v_exp_f32 v139, v139\n"
      "v_exp_f32 v140, v140\n"
      "v_exp_f32 v141, v141\n"
      "s_waitcnt lgkmcnt(0)\n"
      "v_exp_f32 v110, v110\n"
      "v_exp_f32 v111, v111\n"
      "v_smfmac_f32_32x32x32_bf16 v[94:109], v[30:33], v[134:141], v44\n"
      "v_exp_f32 v112, v112\n"
      "v_exp_f32 v113, v113\n"
      "v_exp_f32 v114, v114\n"
      "v_mfma_f32_32x32x16_bf16 v[126:141], v[22:25], v[14:17], v[62:77]\n"
      "v_exp_f32 v115, v115\n"
      "v_exp_f32 v116, v116\n"
      "v_exp_f32 v117, v117\n"
      "v_exp_f32 v118, v118\n"
      "v_exp_f32 v119, v119\n"
      "v_smfmac_f32_32x32x32_bf16 v[78:93], v[34:37], v[110:117], v44\n"
      "v_exp_f32 v120, v120\n"
      "v_exp_f32 v121, v121\n"
      "v_exp_f32 v122, v122\n"
      "v_exp_f32 v123, v123\n"
      "v_exp_f32 v124, v124\n"
      "v_exp_f32 v125, v125\n"
      "ds_read_b64_tr_b16 v[26:27], v2 offset:2048\n"
      "ds_read_b64_tr_b16 v[28:29], v2 offset:2176\n"
      "ds_read_b64_tr_b16 v[30:31], v2 offset:2560\n"
      "ds_read_b64_tr_b16 v[32:33], v2 offset:2688\n"
      "ds_read_b128 v[22:25], v1 offset:27648\n"
      "v_exp_f32 v126, v126\n"
      "v_exp_f32 v127, v127\n"
      "v_smfmac_f32_32x32x32_bf16 v[78:93], v[38:41], v[118:125], v44\n"
      "v_exp_f32 v128, v128\n"
      "v_exp_f32 v129, v129\n"
      "v_exp_f32 v130, v130\n"
      "v_mfma_f32_32x32x16_bf16 v[110:125], v[18:21], v[10:13], v[46:61]\n"
      "v_exp_f32 v131, v131\n"
      "v_exp_f32 v132, v132\n"
      "v_exp_f32 v133, v133\n"
      "v_exp_f32 v134, v134\n"
      "v_exp_f32 v135, v135\n"
      "v_smfmac_f32_32x32x32_bf16 v[94:109], v[34:37], v[126:133], v44\n"
      "v_exp_f32 v136, v136\n"
      "v_exp_f32 v137, v137\n"
      "v_exp_f32 v138, v138\n"
      "v_exp_f32 v139, v139\n"
      "v_exp_f32 v140, v140\n"
      "v_exp_f32 v141, v141\n"
      "s_waitcnt lgkmcnt(0)\n"
      "v_exp_f32 v110, v110\n"
      "v_exp_f32 v111, v111\n"
      "v_smfmac_f32_32x32x32_bf16 v[94:109], v[38:41], v[134:141], v44\n"
      "v_exp_f32 v112, v112\n"
      "v_exp_f32 v113, v113\n"
      "v_exp_f32 v114, v114\n"
      "v_mfma_f32_32x32x16_bf16 v[126:141], v[18:21], v[14:17], v[62:77]\n"
      "v_exp_f32 v115, v115\n"
      "v_exp_f32 v116, v116\n"
      "v_exp_f32 v117, v117\n"
      "v_exp_f32 v118, v118\n"
      "v_exp_f32 v119, v119\n"
      "v_smfmac_f32_32x32x32_bf16 v[78:93], v[26:29], v[110:117], v44\n"
      "v_exp_f32 v120, v120\n"
      "v_exp_f32 v121, v121\n"
      "v_exp_f32 v122, v122\n"
      "v_exp_f32 v123, v123\n"
      "v_exp_f32 v124, v124\n"
      "v_exp_f32 v125, v125\n"
      "ds_read_b64_tr_b16 v[34:35], v2 offset:3072\n"
      "ds_read_b64_tr_b16 v[36:37], v2 offset:3200\n"
      "ds_read_b64_tr_b16 v[38:39], v2 offset:3584\n"
      "ds_read_b64_tr_b16 v[40:41], v2 offset:3712\n"
      "ds_read_b128 v[18:21], v1 offset:28672\n"
      "v_exp_f32 v126, v126\n"
      "v_exp_f32 v127, v127\n"
      "v_smfmac_f32_32x32x32_bf16 v[78:93], v[30:33], v[118:125], v44\n"
      "v_exp_f32 v128, v128\n"
      "v_exp_f32 v129, v129\n"
      "v_exp_f32 v130, v130\n"
      "v_mfma_f32_32x32x16_bf16 v[110:125], v[22:25], v[10:13], v[46:61]\n"
      "v_exp_f32 v131, v131\n"
      "v_exp_f32 v132, v132\n"
      "v_exp_f32 v133, v133\n"
      "v_exp_f32 v134, v134\n"
      "v_exp_f32 v135, v135\n"
      "v_smfmac_f32_32x32x32_bf16 v[94:109], v[26:29], v[126:133], v44\n"
      "v_exp_f32 v136, v136\n"
      "v_exp_f32 v137, v137\n"
      "v_exp_f32 v138, v138\n"
      "v_exp_f32 v139, v139\n"
      "v_exp_f32 v140, v140\n"
      "v_exp_f32 v141, v141\n"
      "s_waitcnt lgkmcnt(0)\n"
      "v_exp_f32 v110, v110\n"
      "v_exp_f32 v111, v111\n"
      "v_smfmac_f32_32x32x32_bf16 v[94:109], v[30:33], v[134:141], v44\n"
      "v_exp_f32 v112, v112\n"
      "v_exp_f32 v113, v113\n"
      "v_exp_f32 v114, v114\n"
      "v_mfma_f32_32x32x16_bf16 v[126:141], v[22:25], v[14:17], v[62:77]\n"
      "v_exp_f32 v115, v115\n"
      "v_exp_f32 v116, v116\n"
      "v_exp_f32 v117, v117\n"
      "v_exp_f32 v118, v118\n"
      "v_exp_f32 v119, v119\n"
      "v_smfmac_f32_32x32x32_bf16 v[78:93], v[34:37], v[110:117], v44\n"
      "v_exp_f32 v120, v120\n"
      "v_exp_f32 v121, v121\n"
      "v_exp_f32 v122, v122\n"
      "v_exp_f32 v123, v123\n"
      "v_exp_f32 v124, v124\n"
      "v_exp_f32 v125, v125\n"
      "ds_read_b64_tr_b16 v[26:27], v2 offset:4096\n"
      "ds_read_b64_tr_b16 v[28:29], v2 offset:4224\n"
      "ds_read_b64_tr_b16 v[30:31], v2 offset:4608\n"
      "ds_read_b64_tr_b16 v[32:33], v2 offset:4736\n"
      "ds_read_b128 v[22:25], v1 offset:29696\n"
      "v_exp_f32 v126, v126\n"
      "v_exp_f32 v127, v127\n"
      "v_smfmac_f32_32x32x32_bf16 v[78:93], v[38:41], v[118:125], v44\n"
      "v_exp_f32 v128, v128\n"
      "v_exp_f32 v129, v129\n"
      "v_exp_f32 v130, v130\n"
      "v_mfma_f32_32x32x16_bf16 v[110:125], v[18:21], v[10:13], v[46:61]\n"
      "v_exp_f32 v131, v131\n"
      "v_exp_f32 v132, v132\n"
      "v_exp_f32 v133, v133\n"
      "v_exp_f32 v134, v134\n"
      "v_exp_f32 v135, v135\n"
      "v_smfmac_f32_32x32x32_bf16 v[94:109], v[34:37], v[126:133], v44\n"
      "v_exp_f32 v136, v136\n"
      "v_exp_f32 v137, v137\n"
      "v_exp_f32 v138, v138\n"
      "v_exp_f32 v139, v139\n"
      "v_exp_f32 v140, v140\n"
      "v_exp_f32 v141, v141\n"
      "s_waitcnt lgkmcnt(0)\n"
      "v_exp_f32 v110, v110\n"
      "v_exp_f32 v111, v111\n"
      "v_smfmac_f32_32x32x32_bf16 v[94:109], v[38:41], v[134:141], v44\n"
      "v_exp_f32 v112, v112\n"
      "v_exp_f32 v113, v113\n"
      "v_exp_f32 v114, v114\n"
      "v_mfma_f32_32x32x16_bf16 v[126:141], v[18:21], v[14:17], v[62:77]\n"
      "v_exp_f32 v115, v115\n"
      "v_exp_f32 v116, v116\n"
      "v_exp_f32 v117, v117\n"
      "v_exp_f32 v118, v118\n"
      "v_exp_f32 v119, v119\n"
      "v_smfmac_f32_32x32x32_bf16 v[78:93], v[26:29], v[110:117], v44\n"
      "v_exp_f32 v120, v120\n"
      "v_exp_f32 v121, v121\n"
      "v_exp_f32 v122, v122\n"
      "v_exp_f32 v123, v123\n"
      "v_exp_f32 v124, v124\n"
      "v_exp_f32 v125, v125\n"
      "ds_read_b64_tr_b16 v[34:35], v2 offset:5120\n"
      "ds_read_b64_tr_b16 v[36:37], v2 offset:5248\n"
      "ds_read_b64_tr_b16 v[38:39], v2 offset:5632\n"
      "ds_read_b64_tr_b16 v[40:41], v2 offset:5760\n"
      "ds_read_b128 v[18:21], v1 offset:30720\n"
      "v_exp_f32 v126, v126\n"
      "v_exp_f32 v127, v127\n"
      "v_smfmac_f32_32x32x32_bf16 v[78:93], v[30:33], v[118:125], v44\n"
      "v_exp_f32 v128, v128\n"
      "v_exp_f32 v129, v129\n"
      "v_exp_f32 v130, v130\n"
      "v_mfma_f32_32x32x16_bf16 v[110:125], v[22:25], v[10:13], v[46:61]\n"
      "v_exp_f32 v131, v131\n"
      "v_exp_f32 v132, v132\n"
      "v_exp_f32 v133, v133\n"
      "v_exp_f32 v134, v134\n"
      "v_exp_f32 v135, v135\n"
      "v_smfmac_f32_32x32x32_bf16 v[94:109], v[26:29], v[126:133], v44\n"
      "v_exp_f32 v136, v136\n"
      "v_exp_f32 v137, v137\n"
      "v_exp_f32 v138, v138\n"
      "v_exp_f32 v139, v139\n"
      "v_exp_f32 v140, v140\n"
      "v_exp_f32 v141, v141\n"
      "s_waitcnt lgkmcnt(0)\n"
      "v_exp_f32 v110, v110\n"
      "v_exp_f32 v111, v111\n"
      "v_smfmac_f32_32x32x32_bf16 v[94:109], v[30:33], v[134:141], v44\n"
      "v_exp_f32 v112, v112\n"
      "v_exp_f32 v113, v113\n"
      "v_exp_f32 v114, v114\n"
      "v_mfma_f32_32x32x16_bf16 v[126:141], v[22:25], v[14:17], v[62:77]\n"
      "v_exp_f32 v115, v115\n"
      "v_exp_f32 v116, v116\n"
      "v_exp_f32 v117, v117\n"
      "v_exp_f32 v118, v118\n"
      "v_exp_f32 v119, v119\n"
      "v_smfmac_f32_32x32x32_bf16 v[78:93], v[34:37], v[110:117], v44\n"
      "v_exp_f32 v120, v120\n"
      "v_exp_f32 v121, v121\n"
      "v_exp_f32 v122, v122\n"
      "v_exp_f32 v123, v123\n"
      "v_exp_f32 v124, v124\n"
      "v_exp_f32 v125, v125\n"
      "ds_read_b64_tr_b16 v[26:27], v2 offset:6144\n"
      "ds_read_b64_tr_b16 v[28:29], v2 offset:6272\n"
      "ds_read_b64_tr_b16 v[30:31], v2 offset:6656\n"
      "ds_read_b64_tr_b16 v[32:33], v2 offset:6784\n"
      "ds_read_b128 v[22:25], v1 offset:31744\n"
      "v_exp_f32 v126, v126\n"
      "v_exp_f32 v127, v127\n"
      "v_smfmac_f32_32x32x32_bf16 v[78:93], v[38:41], v[118:125], v44\n"
      "v_exp_f32 v128, v128\n"
      "v_exp_f32 v129, v129\n"
      "v_exp_f32 v130, v130\n"
      "v_mfma_f32_32x32x16_bf16 v[110:125], v[18:21], v[10:13], v[46:61]\n"
      "v_exp_f32 v131, v131\n"
      "v_exp_f32 v132, v132\n"
      "v_exp_f32 v133, v133\n"
      "v_exp_f32 v134, v134\n"
      "v_exp_f32 v135, v135\n"
      "v_smfmac_f32_32x32x32_bf16 v[94:109], v[34:37], v[126:133], v44\n"
      "v_exp_f32 v136, v136\n"
      "v_exp_f32 v137, v137\n"
      "v_exp_f32 v138, v138\n"
      "v_exp_f32 v139, v139\n"
      "v_exp_f32 v140, v140\n"
      "v_exp_f32 v141, v141\n"
      "s_waitcnt lgkmcnt(0)\n"
      "s_setprio 1\n"
      "v_exp_f32 v110, v110\n"
      "v_exp_f32 v111, v111\n"
      "v_smfmac_f32_32x32x32_bf16 v[94:109], v[38:41], v[134:141], v44\n"
      "v_exp_f32 v112, v112\n"
      "v_exp_f32 v113, v113\n"
      "v_exp_f32 v114, v114\n"
      "v_mfma_f32_32x32x16_bf16 v[126:141], v[22:25], v[10:13], v[46:61]\n"
      "v_exp_f32 v115, v115\n"
      "v_exp_f32 v116, v116\n"
      "v_exp_f32 v117, v117\n"
      "v_exp_f32 v118, v118\n"
      "v_exp_f32 v119, v119\n"
      "v_smfmac_f32_32x32x32_bf16 v[78:93], v[26:29], v[110:117], v44\n"
      "v_exp_f32 v120, v120\n"
      "v_exp_f32 v121, v121\n"
      "v_exp_f32 v122, v122\n"
      "v_exp_f32 v123, v123\n"
      "v_exp_f32 v124, v124\n"
      "v_exp_f32 v125, v125\n"
      "ds_read_b64_tr_b16 v[34:35], v2 offset:7168\n"
      "ds_read_b64_tr_b16 v[36:37], v2 offset:7296\n"
      "ds_read_b64_tr_b16 v[38:39], v2 offset:7680\n"
      "ds_read_b64_tr_b16 v[40:41], v2 offset:7808\n"
      "v_exp_f32 v126, v126\n"
      "v_exp_f32 v127, v127\n"
      "v_smfmac_f32_32x32x32_bf16 v[78:93], v[30:33], v[118:125], v44\n"
      "v_exp_f32 v128, v128\n"
      "v_exp_f32 v129, v129\n"
      "v_exp_f32 v130, v130\n"
      "v_mfma_f32_32x32x16_bf16 v[110:125], v[18:21], v[14:17], v[62:77]\n"
      "v_exp_f32 v131, v131\n"
      "v_exp_f32 v132, v132\n"
      "v_exp_f32 v133, v133\n"
      "v_exp_f32 v134, v134\n"
      "v_exp_f32 v135, v135\n"
      "s_waitcnt lgkmcnt(0)\n"
      "v_smfmac_f32_32x32x32_bf16 v[78:93], v[34:37], v[126:133], v44\n"
      "v_exp_f32 v136, v136\n"
      "v_exp_f32 v137, v137\n"
      "v_exp_f32 v138, v138\n"
      "v_exp_f32 v139, v139\n"
      "v_exp_f32 v140, v140\n"
      "v_exp_f32 v141, v141\n"
      "s_setprio 0\n"
      "v_exp_f32 v110, v110\n"
      "v_exp_f32 v111, v111\n"
      "v_smfmac_f32_32x32x32_bf16 v[78:93], v[38:41], v[134:141], v44\n"
      "v_exp_f32 v112, v112\n"
      "v_exp_f32 v113, v113\n"
      "v_exp_f32 v114, v114\n"
      "v_mfma_f32_32x32x16_bf16 v[126:141], v[22:25], v[14:17], v[62:77]\n"
      "v_exp_f32 v115, v115\n"
      "v_exp_f32 v116, v116\n"
      "v_exp_f32 v117, v117\n"
      "v_exp_f32 v118, v118\n"
      "v_exp_f32 v119, v119\n"
      "v_mov_b32 v4, 2\n"
      "v_smfmac_f32_32x32x32_bf16 v[94:109], v[26:29], v[110:117], v44\n"
      "v_exp_f32 v120, v120\n"
      "v_exp_f32 v121, v121\n"
      "ds_write_b128 v142, v[78:81]\n"
      "ds_write_b128 v142, v[82:85] offset:1024\n"
      "ds_write_b32 v143, v86 offset:2048\n"
      "ds_write_b32 v148, v4 offset:128\n"
      "v_exp_f32 v122, v122\n"
      "v_exp_f32 v123, v123\n"
      "v_exp_f32 v124, v124\n"
      "v_exp_f32 v125, v125\n"
      "v_exp_f32 v126, v126\n"
      "v_exp_f32 v127, v127\n"
      "v_smfmac_f32_32x32x32_bf16 v[94:109], v[30:33], v[118:125], v44\n"
      "v_exp_f32 v128, v128\n"
      "v_exp_f32 v129, v129\n"
      "v_exp_f32 v130, v130\n"
      "v_exp_f32 v131, v131\n"
      "v_exp_f32 v132, v132\n"
      "v_exp_f32 v133, v133\n"
      "v_exp_f32 v134, v134\n"
      "v_exp_f32 v135, v135\n"
      "v_smfmac_f32_32x32x32_bf16 v[94:109], v[34:37], v[126:133], v44\n"
      "ds_read_b32 v8, v149 offset:128\n"
      "ds_read_b32 v18, v145 offset:2048\n"
      "ds_read_b128 v[46:49], v144\n"
      "ds_read_b128 v[50:53], v144 offset:1024\n"
      "v_exp_f32 v136, v136\n"
      "v_exp_f32 v137, v137\n"
      "v_exp_f32 v138, v138\n"
      "v_exp_f32 v139, v139\n"
      "v_exp_f32 v140, v140\n"
      "v_exp_f32 v141, v141\n"
      "s_nop 1\n"
      "v_smfmac_f32_32x32x32_bf16 v[94:109], v[38:41], v[134:141], v44\n"
      "s_waitcnt lgkmcnt(0)\n"
      "v_readfirstlane_b32 s46, v8\n"
      "s_cmp_eq_u32 s46, 2\n"
      "s_cbranch_scc0 Lsd_%=\n"
      "Lbd_%=:\n"
      "s_mov_b32 s46, 0x7149f2ca\n"
      "s_nop 6\n"
      "v_add_f32 v102, v18, v102\n"
      "v_rcp_f32 v22, v102\n"
      "v_pk_add_f32 v[94:95], v[94:95], v[46:47]\n"
      "v_pk_add_f32 v[96:97], v[96:97], v[48:49]\n"
      "v_pk_add_f32 v[98:99], v[98:99], v[50:51]\n"
      "v_pk_add_f32 v[100:101], v[100:101], v[52:53]\n"
      "Lfinal%=:\n"
      "v_pk_mul_f32 v[62:63], v[94:95], v[22:23] op_sel_hi:[1,0]\n"
      "v_pk_mul_f32 v[64:65], v[96:97], v[22:23] op_sel_hi:[1,0]\n"
      "v_pk_mul_f32 v[66:67], v[98:99], v[22:23] op_sel_hi:[1,0]\n"
      "v_pk_mul_f32 v[68:69], v[100:101], v[22:23] op_sel_hi:[1,0]\n"
      "v_cvt_pk_bf16_f32 v10, v62, v63\n"
      "v_cvt_pk_bf16_f32 v12, v66, v67\n"
      "v_cvt_pk_bf16_f32 v11, v64, v65\n"
      "v_cvt_pk_bf16_f32 v13, v68, v69\n"
      "v_maximum3_f32 v27, |v97|, |v98|, |v99|\n"
      "v_permlane32_swap_b32 v10, v12\n"
      "v_permlane32_swap_b32 v11, v13\n"
      "global_store_dwordx4 v5, v[10:13], s[26:27] sc0 sc1\n"
      "v_maximum3_f32 v26, |v94|, |v95|, |v96|\n"
      "v_maximum3_f32 v28, |v100|, |v101|, |v102|\n"
      "v_maximum3_f32 v26, v26, v27, v28\n"
      "v_maximum3_f32 v26, v26, |v22|, |v22|\n"
      "v_cmp_ngt_f32 vcc, s46, v26\n"
      "s_or_b32 s47, vcc_lo, vcc_hi\n"
      "s_cbranch_scc1 Lfin%=\n"
      "s_endpgm\n"
      "Lsa_%=:\n"
      "s_sleep 1\n"
      "ds_read_b32 v8, v6 offset:0\n"
      "ds_read_b128 v[22:25], v1 offset:1024\n"
      "ds_read_b64_tr_b16 v[26:27], v2 offset:0\n"
      "ds_read_b64_tr_b16 v[28:29], v2 offset:128\n"
      "ds_read_b64_tr_b16 v[30:31], v2 offset:512\n"
      "ds_read_b64_tr_b16 v[32:33], v2 offset:640\n"
      "ds_read_b64_tr_b16 v[34:35], v2 offset:1024\n"
      "ds_read_b64_tr_b16 v[36:37], v2 offset:1152\n"
      "ds_read_b64_tr_b16 v[38:39], v2 offset:1536\n"
      "ds_read_b64_tr_b16 v[40:41], v2 offset:1664\n"
      "s_waitcnt lgkmcnt(0)\n"
      "v_readfirstlane_b32 s46, v8\n"
      "s_cmp_eq_u32 s46, 257\n"
      "s_cbranch_scc0 Lsa_%=\n"
      "s_branch Lba_%=\n"
      "Lsb_%=:\n"
      "s_sleep 1\n"
      "ds_read_b32 v8, v149 offset:128\n"
      "ds_read_b128 v[14:17], v142\n"
      "ds_read_b128 v[62:65], v147 offset:1024\n"
      "ds_read_b128 v[66:69], v147 offset:1024\n"
      "ds_read_b128 v[70:73], v147 offset:1024\n"
      "ds_read_b128 v[74:77], v147 offset:1024\n"
      "s_waitcnt lgkmcnt(0)\n"
      "v_readfirstlane_b32 s46, v8\n"
      "s_cmp_ge_u32 s46, 1\n"
      "s_cbranch_scc0 Lsb_%=\n"
      "s_branch Lbb_%=\n"
      "Lsc_%=:\n"
      "s_sleep 1\n"
      "ds_read_b32 v8, v6 offset:4\n"
      "ds_read_b128 v[18:21], v1 offset:2048\n"
      "s_waitcnt lgkmcnt(0)\n"
      "v_readfirstlane_b32 s46, v8\n"
      "s_cmp_eq_u32 s46, 257\n"
      "s_cbranch_scc0 Lsc_%=\n"
      "s_branch Lbc_%=\n"
      "Lpm2_%=:\n"
      "ds_read_b32 v8, v6 offset:8\n"
      "s_waitcnt lgkmcnt(0)\n"
      "v_readfirstlane_b32 s46, v8\n"
      "s_cmp_eq_u32 s46, 257\n"
      "s_cbranch_scc1 Lrm2_%=\n"
      "s_sleep 1\n"
      "s_branch Lpm2_%=\n"
      "Lpm3_%=:\n"
      "ds_read_b32 v8, v6 offset:12\n"
      "s_waitcnt lgkmcnt(0)\n"
      "v_readfirstlane_b32 s46, v8\n"
      "s_cmp_eq_u32 s46, 257\n"
      "s_cbranch_scc1 Lrm3_%=\n"
      "s_sleep 1\n"
      "s_branch Lpm3_%=\n"
      "Lpm4_%=:\n"
      "ds_read_b32 v8, v6 offset:16\n"
      "s_waitcnt lgkmcnt(0)\n"
      "v_readfirstlane_b32 s46, v8\n"
      "s_cmp_eq_u32 s46, 257\n"
      "s_cbranch_scc1 Lrm4_%=\n"
      "s_sleep 1\n"
      "s_branch Lpm4_%=\n"
      "Lpm5_%=:\n"
      "ds_read_b32 v8, v6 offset:20\n"
      "s_waitcnt lgkmcnt(0)\n"
      "v_readfirstlane_b32 s46, v8\n"
      "s_cmp_eq_u32 s46, 257\n"
      "s_cbranch_scc1 Lrm5_%=\n"
      "s_sleep 1\n"
      "s_branch Lpm5_%=\n"
      "Lpm6_%=:\n"
      "ds_read_b32 v8, v6 offset:24\n"
      "s_waitcnt lgkmcnt(0)\n"
      "v_readfirstlane_b32 s46, v8\n"
      "s_cmp_eq_u32 s46, 257\n"
      "s_cbranch_scc1 Lrm6_%=\n"
      "s_sleep 1\n"
      "s_branch Lpm6_%=\n"
      "Lpm7_%=:\n"
      "ds_read_b32 v8, v6 offset:28\n"
      "s_waitcnt lgkmcnt(0)\n"
      "v_readfirstlane_b32 s46, v8\n"
      "s_cmp_eq_u32 s46, 257\n"
      "s_cbranch_scc1 Lrm7_%=\n"
      "s_sleep 1\n"
      "s_branch Lpm7_%=\n"
      "Lsd_%=:\n"
      "s_sleep 1\n"
      "ds_read_b32 v8, v149 offset:128\n"
      "ds_read_b32 v18, v145 offset:2048\n"
      "ds_read_b128 v[46:49], v144\n"
      "ds_read_b128 v[50:53], v144 offset:1024\n"
      "s_waitcnt lgkmcnt(0)\n"
      "v_readfirstlane_b32 s46, v8\n"
      "s_cmp_eq_u32 s46, 2\n"
      "s_cbranch_scc0 Lsd_%=\n"
      "s_branch Lbd_%=\n"
      "Lldr2%=:\n"
      "s_waitcnt vmcnt(3)\n"
      "ds_write_b128 v1, v[32:35] offset:4096\n"
      "s_add_u32 s48, s48, 4096\n"
      "s_addc_u32 s49, s49, 0\n"
      "global_load_dwordx4 v[48:51], v4, s[48:49] offset:0\n"
      "s_sleep 3\n"
      "s_waitcnt vmcnt(3)\n"
      "ds_write_b128 v1, v[36:39] offset:5120\n"
      "ds_write_b8 v6, v8 offset:8\n"
      "global_load_dwordx4 v[52:55], v4, s[48:49] offset:1024\n"
      "s_sleep 3\n"
      "s_waitcnt vmcnt(3)\n"
      "ds_write_b128 v1, v[40:43] offset:6144\n"
      "global_load_dwordx4 v[56:59], v4, s[48:49] offset:2048\n"
      "s_sleep 3\n"
      "s_waitcnt vmcnt(3)\n"
      "ds_write_b128 v1, v[44:47] offset:7168\n"
      "global_load_dwordx4 v[60:63], v4, s[48:49] offset:3072\n"
      "s_sleep 3\n"
      "s_waitcnt vmcnt(3)\n"
      "ds_write_b128 v1, v[48:51] offset:8192\n"
      "ds_write_b8 v6, v8 offset:12\n"
      "s_add_u32 s48, s48, 4096\n"
      "s_addc_u32 s49, s49, 0\n"
      "global_load_dwordx4 v[64:67], v4, s[48:49] offset:0\n"
      "s_sleep 3\n"
      "s_waitcnt vmcnt(3)\n"
      "ds_write_b128 v1, v[52:55] offset:9216\n"
      "global_load_dwordx4 v[68:71], v4, s[48:49] offset:1024\n"
      "s_sleep 3\n"
      "s_waitcnt vmcnt(3)\n"
      "ds_write_b128 v1, v[56:59] offset:10240\n"
      "global_load_dwordx4 v[72:75], v4, s[48:49] offset:2048\n"
      "s_sleep 3\n"
      "s_waitcnt vmcnt(3)\n"
      "ds_write_b128 v1, v[60:63] offset:11264\n"
      "global_load_dwordx4 v[76:79], v4, s[48:49] offset:3072\n"
      "s_sleep 3\n"
      "s_waitcnt vmcnt(3)\n"
      "ds_write_b128 v1, v[64:67] offset:12288\n"
      "ds_write_b8 v6, v8 offset:16\n"
      "s_add_u32 s48, s48, 4096\n"
      "s_addc_u32 s49, s49, 0\n"
      "global_load_dwordx4 v[80:83], v4, s[48:49] offset:0\n"
      "s_sleep 3\n"
      "s_waitcnt vmcnt(3)\n"
      "ds_write_b128 v1, v[68:71] offset:13312\n"
      "global_load_dwordx4 v[84:87], v4, s[48:49] offset:1024\n"
      "s_sleep 3\n"
      "s_waitcnt vmcnt(3)\n"
      "ds_write_b128 v1, v[72:75] offset:14336\n"
      "global_load_dwordx4 v[88:91], v4, s[48:49] offset:2048\n"
      "s_sleep 3\n"
      "s_waitcnt vmcnt(3)\n"
      "ds_write_b128 v1, v[76:79] offset:15360\n"
      "global_load_dwordx4 v[92:95], v4, s[48:49] offset:3072\n"
      "s_sleep 3\n"
      "s_waitcnt vmcnt(3)\n"
      "ds_write_b128 v1, v[80:83] offset:16384\n"
      "s_add_u32 s48, s48, 4096\n"
      "s_addc_u32 s49, s49, 0\n"
      "global_load_dwordx4 v[96:99], v4, s[48:49] offset:0\n"
      "s_sleep 3\n"
      "s_waitcnt vmcnt(3)\n"
      "ds_write_b128 v1, v[84:87] offset:17408\n"
      "global_load_dwordx4 v[100:103], v4, s[48:49] offset:1024\n"
      "s_sleep 3\n"
      "s_waitcnt vmcnt(3)\n"
      "ds_write_b128 v1, v[88:91] offset:18432\n"
      "ds_write_b8 v6, v8 offset:20\n"
      "global_load_dwordx4 v[104:107], v4, s[48:49] offset:2048\n"
      "s_sleep 3\n"
      "s_waitcnt vmcnt(3)\n"
      "ds_write_b128 v1, v[92:95] offset:19456\n"
      "global_load_dwordx4 v[108:111], v4, s[48:49] offset:3072\n"
      "s_sleep 3\n"
      "s_waitcnt vmcnt(3)\n"
      "ds_write_b128 v1, v[96:99] offset:20480\n"
      "s_add_u32 s48, s48, 4096\n"
      "s_addc_u32 s49, s49, 0\n"
      "global_load_dwordx4 v[112:115], v4, s[48:49] offset:0\n"
      "s_sleep 3\n"
      "s_waitcnt vmcnt(3)\n"
      "ds_write_b128 v1, v[100:103] offset:21504\n"
      "global_load_dwordx4 v[116:119], v4, s[48:49] offset:1024\n"
      "s_sleep 3\n"
      "s_waitcnt vmcnt(3)\n"
      "ds_write_b128 v1, v[104:107] offset:22528\n"
      "global_load_dwordx4 v[120:123], v4, s[48:49] offset:2048\n"
      "s_sleep 3\n"
      "s_waitcnt vmcnt(3)\n"
      "ds_write_b128 v1, v[108:111] offset:23552\n"
      "global_load_dwordx4 v[124:127], v4, s[48:49] offset:3072\n"
      "s_sleep 3\n"
      "s_waitcnt vmcnt(3)\n"
      "ds_write_b128 v1, v[112:115] offset:24576\n"
      "s_add_u32 s48, s48, 4096\n"
      "s_addc_u32 s49, s49, 0\n"
      "global_load_dwordx4 v[128:131], v4, s[48:49] offset:0\n"
      "s_sleep 3\n"
      "s_waitcnt vmcnt(3)\n"
      "ds_write_b128 v1, v[116:119] offset:25600\n"
      "ds_write_b8 v6, v8 offset:24\n"
      "global_load_dwordx4 v[132:135], v4, s[48:49] offset:1024\n"
      "s_sleep 3\n"
      "s_waitcnt vmcnt(3)\n"
      "ds_write_b128 v1, v[120:123] offset:26624\n"
      "global_load_dwordx4 v[136:139], v4, s[48:49] offset:2048\n"
      "s_sleep 3\n"
      "s_waitcnt vmcnt(3)\n"
      "ds_write_b128 v1, v[124:127] offset:27648\n"
      "global_load_dwordx4 v[140:143], v4, s[48:49] offset:3072\n"
      "s_sleep 3\n"
      "s_waitcnt vmcnt(3)\n"
      "ds_write_b128 v1, v[128:131] offset:28672\n"
      "s_sleep 3\n"
      "s_waitcnt vmcnt(2)\n"
      "ds_write_b128 v1, v[132:135] offset:29696\n"
      "s_sleep 3\n"
      "s_waitcnt vmcnt(1)\n"
      "ds_write_b128 v1, v[136:139] offset:30720\n"
      "s_sleep 3\n"
      "s_waitcnt vmcnt(0)\n"
      "ds_write_b128 v1, v[140:143] offset:31744\n"
      "ds_write_b8 v6, v8 offset:28\n"
      "s_waitcnt vmcnt(0) lgkmcnt(0)\n"
      "s_endpgm\n"
      "Lfin%=:\n"
      "s_waitcnt vmcnt(0) lgkmcnt(0)\n"
      : "=&{v5}"(qoff), "=&{v9}"(lane), "=&{s[20:21]}"(qp), "=&{s[26:27]}"(op), "=&{s40}"(wid), "=&{s47}"(bad)
      : [karg] "s"(karg), [bid] "s"(bid), [lds0] "s"(lds0), "{v0}"(tid)
      : "v1", "v2", "v3", "v4", "v6", "v7", "v8", "v10", "v11", "v12", "v13", "v14", "v15", "v16", "v17", "v18", "v19", "v20", "v21", "v22", "v23", "v24", "v25", "v26", "v27", "v28", "v29", "v30", "v31", "v32", "v33", "v34", "v35", "v36", "v37", "v38", "v39", "v40", "v41", "v42", "v43", "v44", "v45", "v46", "v47", "v48", "v49", "v50", "v51", "v52", "v53", "v54", "v55", "v56", "v57", "v58", "v59", "v60", "v61", "v62", "v63", "v64", "v65", "v66", "v67", "v68", "v69", "v70", "v71", "v72", "v73", "v74", "v75", "v76", "v77", "v78", "v79", "v80", "v81", "v82", "v83", "v84", "v85", "v86", "v87", "v88", "v89", "v90", "v91", "v92", "v93", "v94", "v95", "v96", "v97", "v98", "v99", "v100", "v101", "v102", "v103", "v104", "v105", "v106", "v107", "v108", "v109", "v110", "v111", "v112", "v113", "v114", "v115", "v116", "v117", "v118", "v119", "v120", "v121", "v122", "v123", "v124", "v125", "v126", "v127", "v128", "v129", "v130", "v131", "v132", "v133", "v134", "v135", "v136", "v137", "v138", "v139", "v140", "v141", "v142", "v143", "v144", "v145", "v146", "v147", "v148", "v149", "s22", "s23", "s24", "s25", "s41", "s42", "s43", "s44", "s45", "s46", "s48", "s49", "s52", "s53", "s54", "s55", "vcc", "scc", "memory");
  if (bad) exact_store(qp + qoff, op + qoff, (lds_cptr)lds, lane);
}

extern "C" void kernel_launch(void* const* d_in, const int* in_sizes, int n_in, void* d_out, int out_size, void* d_ws, size_t ws_size, hipStream_t stream) {
  attn_kernel<<<256, 768, 0, stream>>>((const char*)d_in[0], (const char*)d_in[1], (const char*)d_in[2], (char*)d_out);
}
```
